```python
import torch

def _softmax(x, axis=-1):
    return torch.softmax(x, dim=axis)
def _gelu(x):
    return torch.nn.functional.gelu(x, approximate="tanh")
def _silu(x):
    return torch.nn.functional.silu(x)
def _sigmoid(x):
    return torch.sigmoid(x)
def _layernorm(x, axis=-1, eps=1e-5):
    m = x.mean(dim=axis, keepdim=True)
    v = x.var(dim=axis, keepdim=True, unbiased=False)
    return (x - m) / torch.sqrt(v + eps)
def _scatter_add(tgt, idx, src, axis=0):
    return tgt.clone().index_add_(axis, idx, src)
def _segment_sum(src, seg, n):
    out = torch.zeros((n,) + src.shape[1:], dtype=src.dtype)
    return out.index_add_(0, seg, src)
def _pad(x, pw):
    flat = tuple(v for pair in reversed(pw) for v in pair)
    return torch.nn.functional.pad(x, flat)

kernel_name = 'gated_mix__mha_block__mixer_s_mlp__prompt'

def setup_inputs():

    _w = (lambda a: (lambda t: t.to(torch.bfloat16) if t.is_floating_point() else t)(torch.as_tensor(a)))
    def _A_setup():
        B, S, D, H = 1, 2048, 512, 32
        return {
            "x": _w((lambda *s: torch.randn(*s, dtype=torch.bfloat16) * 0.05)(B, S, D)),
            "wqkv": _w((lambda *s: torch.randn(*s, dtype=torch.bfloat16) * 1.6)(D, 3 * D)),
            "wo": _w((lambda *s: torch.randn(*s, dtype=torch.bfloat16) * 0.1)(D, D)),
        }
    _A_in = _A_setup()

    def _B_setup():
        B, S, D, F = 1, 2048, 512, 1792
        return {
            "x":  _w((lambda *s: torch.randn(*s, dtype=torch.bfloat16) * 0.05)(B, S, D)),
            "w1": _w((lambda *s: torch.randn(*s, dtype=torch.bfloat16) * 3.2)(D, D) * (D ** -0.5)),
            "w2": _w((lambda *s: torch.randn(*s, dtype=torch.bfloat16) * 3.2)(D, D) * (D ** -0.5)),
            "w3": _w((lambda *s: torch.randn(*s, dtype=torch.bfloat16) * 3.2)(D, F) * (D ** -0.5)),
            "w4": _w((lambda *s: torch.randn(*s, dtype=torch.bfloat16) * 1.6)(F, D) * (F ** -0.5)),
        }
    _B_in = _B_setup()

    return {
        "A_x": _A_in["x"],
        "A_wqkv": _A_in["wqkv"],
        "A_wo": _A_in["wo"],
        "B_x": _B_in["x"],
        "B_w1": _B_in["w1"],
        "B_w2": _B_in["w2"],
        "B_w3": _B_in["w3"],
        "B_w4": _B_in["w4"],
        "gate_w": _w((lambda *s: torch.randn(*s, dtype=torch.bfloat16) * 0.05)(512) * 16.0),
    }

def reference(A_x, A_wqkv, A_wo, B_x, B_w1, B_w2, B_w3, B_w4, gate_w):
    def _A_ref(x, wqkv, wo):
        B, S, D = x.shape
        H = 32
        Dh = D // H
        qkv = (lambda x, *d: x.permute(*d))((x @ wqkv).reshape(B, S, 3, H, Dh), 2, 0, 3, 1, 4)
        q, k, v = qkv[0], qkv[1], qkv[2]
        scores = torch.einsum("bhqd,bhkd->bhqk", q, k) * (Dh ** -0.5)
        p = _softmax(scores, axis=-1)
        o = torch.einsum("bhqk,bhkd->bhqd", p, v)
        return (lambda x, *d: x.permute(*d))(o, 0, 2, 1, 3).reshape(B, S, D) @ wo

    def _B_ref(x, w1, w2, w3, w4):
        B, S, D = x.shape
        h = _gelu(_layernorm(x, axis=-1) @ w1)
        c = D // 4
        g0 = _pad(h[:, :, 0*c:1*c], ((0, 0), (1, 0), (0, 0)))[:, :S, :]
        g1 = _pad(h[:, :, 1*c:2*c], ((0, 0), (0, 1), (0, 0)))[:, 1:, :]
        g2 = _pad(h[:, :, 2*c:3*c], ((0, 0), (2, 0), (0, 0)))[:, :S, :]
        g3 = _pad(h[:, :, 3*c:   ], ((0, 0), (0, 2), (0, 0)))[:, 2:, :]
        hs = torch.concatenate([g0, g1, g2, g3], axis=-1) @ w2
        x1 = x + hs
        h2 = _gelu(_layernorm(x1, axis=-1) @ w3) @ w4
        return x1 + h2

    x = A_x
    g = _sigmoid((x * gate_w).sum(-1, keepdims=True))
    a = _A_ref(x, A_wqkv, A_wo)
    b = _B_ref(x, B_w1, B_w2, B_w3, B_w4)
    return g * a + (1 - g) * b
```

```cpp
#include <hip/hip_runtime.h>
#include <hip/hip_bf16.h>

typedef __attribute__((ext_vector_type(8))) __bf16 bf16x8;
typedef __attribute__((ext_vector_type(2))) __bf16 bf16x2;
typedef __attribute__((ext_vector_type(16))) float f32x16;
typedef __attribute__((ext_vector_type(4))) float f32x4;
typedef __attribute__((ext_vector_type(2))) float f32x2;
typedef __attribute__((ext_vector_type(4))) unsigned u32x4;
typedef __attribute__((ext_vector_type(2))) unsigned u32x2;
typedef __attribute__((ext_vector_type(4))) short s16x4;
#define DI __device__ __forceinline__
#define LAS __attribute__((address_space(3)))
#define MFMA(a, b, c) __builtin_amdgcn_mfma_f32_32x32x16_bf16(__builtin_bit_cast(bf16x8, a), __builtin_bit_cast(bf16x8, b), c, 0, 0, 0)

constexpr unsigned MB = 1u << 20;
constexpr unsigned O_QF = 0, O_KV = 2 * MB, O_HS = 6 * MB, O_W2 = 8 * MB, O_WO = 8 * MB + MB / 2, O_W3 = 9 * MB, O_W4 = 11 * MB,
                 O_X1 = 13 * MB, O_OF = 15 * MB, O_PART = 17 * MB, O_STAT = 33 * MB, O_KMAX = 34 * MB, O_G = 34 * MB + 16384,
                 O_CNT = 34 * MB + 32768;

DI unsigned pk2(float a, float b) { f32x2 v = {a, b}; return __builtin_bit_cast(unsigned, __builtin_convertvector(v, bf16x2)); }
DI float lo16(unsigned u) { return __uint_as_float(u << 16); }
DI float hi16(unsigned u) { return __uint_as_float(u & 0xffff0000u); }
DI float dot2(unsigned a, unsigned b, float c) { return __builtin_amdgcn_fdot2_f32_bf16(__builtin_bit_cast(bf16x2, a), __builtin_bit_cast(bf16x2, b), c, false); }
DI float xhalf(float v) { return __shfl_xor(v, 32); }
DI float gelu(float x) {
  float z = x * __builtin_fmaf(x * x, -0.10294324f, -2.3022082f);
  return x * __builtin_amdgcn_rcpf(1.0f + __builtin_amdgcn_exp2f(z));
}
DI void dma16(const void* g, unsigned lds_base) {
  unsigned sv;
  asm volatile("s_mov_b32 %0, m0\n\ts_mov_b32 m0, %2\n\ts_nop 0\n\tglobal_load_lds_dwordx4 %1, off\n\ts_mov_b32 m0, %0"
               : "=&s"(sv) : "v"(g), "s"(lds_base) : "memory");
}
#define WAIT_VM(n) asm volatile("s_waitcnt vmcnt(" #n ")" ::: "memory")
#define WAIT_LGKM() asm volatile("s_waitcnt lgkmcnt(0)" ::: "memory")
DI u32x4 pack8(const f32x16& a, int o, float m) {
  u32x4 r = {pk2(a[o] * m, a[o + 1] * m), pk2(a[o + 2] * m, a[o + 3] * m), pk2(a[o + 4] * m, a[o + 5] * m), pk2(a[o + 6] * m, a[o + 7] * m)};
  return r;
}
DI u32x4 ldg16(const char* p) { return *(const u32x4*)p; }
typedef __amdgpu_buffer_rsrc_t rsrc_t;
DI rsrc_t ws_rsrc(const char* ws) { return __builtin_amdgcn_make_buffer_rsrc((void*)ws, (short)0, (int)(35 * MB), 0x00020000); }
#define bld(soff) __builtin_amdgcn_raw_buffer_load_b128(rs, lane * 16, (unsigned)(soff), 0)

constexpr int K1_LDS = 4 * 32768 + 1024 + 1024;

template <int ROLE>
DI void k1_body(char* sm, const char* x, const char* W, int ldw, char* ws, int bm, int bn, int w, int lane, int tid) {
  const int wn = w & 1, wm = w >> 1, r = lane & 31, h = lane >> 5;
  const unsigned smb = (unsigned)(size_t)(LAS char*)sm;
  const int xr = w * 32 + (lane >> 3);
  const char* xg = x + (size_t)(bm * 128 + xr) * 1024;
  const int wr = w * 16 + (lane >> 4);
  const char* wg = W + (size_t)wr * ldw;
  auto issue = [&](int c) {
    const unsigned sb = smb + (c & 3) * 32768 + w * 4096;
#pragma unroll
    for (int p = 0; p < 4; ++p) {
      const int row = xr + 8 * p;
      dma16(xg + p * 8192 + c * 128 + (((lane & 7) ^ ((row >> 1) & 7)) << 4), sb + p * 1024);
    }
#pragma unroll
    for (int p = 0; p < 4; ++p) {
      const int row = wr + 4 * p;
      dma16(wg + (size_t)(c * 64 + 4 * p) * ldw + (((lane & 15) ^ (((row & 3) << 2) | ((row >> 2) & 3))) << 4), sb + 16384 + p * 1024);
    }
  };
  issue(0);
  issue(1);
  issue(2);
  f32x16 acc[2][2];
#pragma unroll
  for (int a = 0; a < 2; ++a)
#pragma unroll
    for (int b = 0; b < 2; ++b)
#pragma unroll
      for (int i = 0; i < 16; ++i) acc[a][b][i] = 0.f;
  float s1[2] = {0.f, 0.f}, s2[2] = {0.f, 0.f}, cs[2] = {0.f, 0.f};
  const int xrow = wm * 64 + r;
  const int xsw = (xrow >> 1) & 7;
  const int blk = (lane >> 4) & 1, q = (lane & 15) >> 2, pp = lane & 3;
  for (int c = 0; c < 8; ++c) {
    if (c < 6) WAIT_VM(16); else if (c == 6) WAIT_VM(8); else WAIT_VM(0);
    __builtin_amdgcn_s_barrier();
    asm volatile("" ::: "memory");
    if (c + 3 < 8) issue(c + 3);
    const char* xs = sm + (c & 3) * 32768;
    const char* wsm = xs + 16384;
#pragma unroll
    for (int ks = 0; ks < 4; ++ks) {
      u32x4 xf[2], wf[2];
#pragma unroll
      for (int rt = 0; rt < 2; ++rt) xf[rt] = *(const u32x4*)(xs + (xrow + rt * 32) * 128 + (((2 * ks + h) ^ xsw) << 4));
#pragma unroll
      for (int nt = 0; nt < 2; ++nt)
#pragma unroll
        for (int t = 0; t < 2; ++t) {
          const int row = 16 * ks + 8 * h + 4 * t + q;
          const int ch = 4 * (wn * 2 + nt) + 2 * blk + (pp >> 1);
          const int off = 256 * row + 16 * (ch ^ (((row & 3) << 2) | ((row >> 2) & 3))) + 8 * (pp & 1);
          u32x2 v = __builtin_bit_cast(u32x2, __builtin_amdgcn_ds_read_tr16_b64_v4i16((LAS s16x4*)(wsm + off)));
          wf[nt][2 * t] = v[0];
          wf[nt][2 * t + 1] = v[1];
        }
#pragma unroll
      for (int nt = 0; nt < 2; ++nt)
#pragma unroll
        for (int rt = 0; rt < 2; ++rt) acc[nt][rt] = ROLE == 1 ? MFMA(xf[rt], wf[nt], acc[nt][rt]) : MFMA(wf[nt], xf[rt], acc[nt][rt]);
      if (ROLE == 2) {
#pragma unroll
        for (int a = 0; a < 2; ++a)
#pragma unroll
          for (int d = 0; d < 4; ++d) {
            s1[a] = dot2(xf[a][d], 0x3f803f80u, s1[a]);
            s2[a] = dot2(xf[a][d], xf[a][d], s2[a]);
            cs[a] = dot2(wf[a][d], 0x3f803f80u, cs[a]);
          }
      }
      if (ROLE == 3) {
        const u32x4 gw = *(const u32x4*)(sm + 131072 + c * 128 + ks * 32 + h * 16);
#pragma unroll
        for (int a = 0; a < 2; ++a)
#pragma unroll
          for (int d = 0; d < 4; ++d) s1[a] = dot2(xf[a][d], gw[d], s1[a]);
      }
    }
  }
  const int tile0 = bm * 4 + wm * 2;
  if (ROLE == 0 || ROLE == 3) {
    const bool isq = bn < 4;
    const float m = isq ? 0.25f * 1.4426950409f : 1.0f;
    const int head0 = (bn & 3) * 8 + wn * 4;
#pragma unroll
    for (int nt = 0; nt < 2; ++nt)
#pragma unroll
      for (int rt = 0; rt < 2; ++rt)
#pragma unroll
        for (int s = 0; s < 2; ++s) {
          const int head = head0 + nt * 2 + s;
          const u32x4 v = pack8(acc[nt][rt], 8 * s, m);
          if (isq) {
            *(u32x4*)(ws + O_QF + (size_t)(head * 64 + tile0 + rt) * 1024 + lane * 16) = v;
          } else {
            *(u32x4*)(ws + O_KV + (size_t)(head * 64 + tile0 + rt) * 2048 + lane * 16) = v;
            float n2 = dot2(v[0], v[0], dot2(v[1], v[1], dot2(v[2], v[2], dot2(v[3], v[3], 0.f))));
            n2 += xhalf(n2);
#pragma unroll
            for (int o = 16; o >= 1; o >>= 1) n2 = fmaxf(n2, __shfl_xor(n2, o));
            if (lane == 0) ((float*)(ws + O_KMAX))[head * 64 + tile0 + rt] = n2;
          }
        }
    if (ROLE == 3 && wn == 0) {
#pragma unroll
      for (int rt = 0; rt < 2; ++rt) {
        const float z = s1[rt] + xhalf(s1[rt]);
        if (h == 0) ((float*)(ws + O_G))[(tile0 + rt) * 32 + r] = 1.0f / (1.0f + __expf(-z));
      }
    }
  } else if (ROLE == 1) {
#pragma unroll
    for (int nt = 0; nt < 2; ++nt)
#pragma unroll
      for (int rt = 0; rt < 2; ++rt)
#pragma unroll
        for (int s = 0; s < 2; ++s) {
          const int head = (bn & 3) * 8 + wn * 4 + nt * 2 + (r >> 4);
          *(u32x4*)(ws + O_KV + (size_t)(head * 64 + tile0 + rt) * 2048 + 1024 + s * 512 + h * 256 + (r & 15) * 16) = pack8(acc[nt][rt], 8 * s, 1.0f);
        }
  } else {
    float* cl = (float*)(sm + 131072) + w * 64;
#pragma unroll
    for (int nt = 0; nt < 2; ++nt) {
      const float t = cs[nt] + xhalf(cs[nt]);
      if (h == 0) cl[nt * 32 + r] = t;
    }
    const int g = bn & 3;
    const int sh = (g & 1) ? -(1 + (g >> 1)) : (1 + (g >> 1));
#pragma unroll
    for (int rt = 0; rt < 2; ++rt) {
      const float mean = (s1[rt] + xhalf(s1[rt])) * (1.0f / 512.0f);
      const float var = fmaxf((s2[rt] + xhalf(s2[rt])) * (1.0f / 512.0f) - mean * mean, 0.f);
      const float rstd = rsqrtf(var + 1e-5f);
      const float nm = -mean * rstd;
      const int rd = (tile0 + rt) * 32 + r + sh;
      const bool ok = rd >= 0 && rd < 2048;
      const int rw = rd & 2047;
#pragma unroll
      for (int nt = 0; nt < 2; ++nt) {
        f32x16 o;
#pragma unroll
        for (int gq = 0; gq < 4; ++gq) {
          const f32x4 c4 = *(const f32x4*)(cl + nt * 32 + 8 * gq + 4 * h);
#pragma unroll
          for (int e = 0; e < 4; ++e) o[4 * gq + e] = gelu(__builtin_fmaf(acc[nt][rt][4 * gq + e], rstd, nm * c4[e]));
        }
#pragma unroll
        for (int s = 0; s < 2; ++s) {
          u32x4 v = pack8(o, 8 * s, 1.0f);
          if (!ok) v = u32x4{0u, 0u, 0u, 0u};
          *(u32x4*)(ws + O_HS + (size_t)((rw >> 5) * 32 + g * 8 + wn * 4 + nt * 2 + s) * 1024 + ((rw & 31) + 32 * h) * 16) = v;
        }
      }
    }
  }
}

__global__ __launch_bounds__(256, 1) void k1(const char* __restrict__ x, const char* __restrict__ wqkv, const char* __restrict__ w1,
                                             const char* __restrict__ wo, const char* __restrict__ w2, const char* __restrict__ w3,
                                             const char* __restrict__ w4, const char* __restrict__ gw, char* __restrict__ ws) {
  extern __shared__ __attribute__((aligned(16))) char sm[];
  const int tid = threadIdx.x, lane = tid & 63, w = __builtin_amdgcn_readfirstlane(tid >> 6);
  const int b = blockIdx.x, xcd = b & 7, i = b >> 3;
  const int bm = (xcd >> 1) * 4 + (i & 3), bn = (xcd & 1) * 8 + (i >> 2);
  const char* W = bn < 12 ? wqkv + bn * 256 : w1 + (bn - 12) * 256;
  const int ldw = bn < 12 ? 3072 : 1024;
  const unsigned smb = (unsigned)(size_t)(LAS char*)sm;
  const int u = (b & 7) * 128 + (b >> 3) * 4 + w;
  const int uu = ((u * 9) >> 4);
  const bool side = (((u + 1) * 9) >> 4) != uu;
  u32x4 in[8];
  char* d = ws;
  if (side) {
    const char* src; char* dst; int ld, ks, nb, nks;
    if (uu < 64) { src = w2; dst = ws + O_W2; ld = 1024; ks = uu >> 1; nb = uu & 1; nks = 32; }
    else if (uu < 128) { src = wo; dst = ws + O_WO; ld = 1024; ks = (uu - 64) >> 1; nb = uu & 1; nks = 32; }
    else if (uu < 352) { src = w3; dst = ws + O_W3; ld = 3584; ks = (uu - 128) / 7; nb = (uu - 128) % 7; nks = 32; }
    else { src = w4; dst = ws + O_W4; ld = 1024; ks = (uu - 352) >> 1; nb = uu & 1; nks = 112; }
    const int r = lane & 31, h = lane >> 5;
#pragma unroll
    for (int j = 0; j < 8; ++j) in[j] = ldg16(src + (size_t)(ks * 16 + 8 * (j >> 2) + 4 * h + (j & 3)) * ld + nb * 512 + r * 16);
    d = dst + (size_t)((nb * 8 + (r >> 2)) * nks + ks) * 1024 + ((r & 3) * 8 + 32 * h) * 16;
  } else {
#pragma unroll
    for (int j = 0; j < 8; ++j) in[j] = u32x4{0u, 0u, 0u, 0u};
  }
  if (b == 0 && tid < 32) __hip_atomic_store((unsigned*)(ws + O_CNT + tid * 256), 0u, __ATOMIC_RELAXED, __HIP_MEMORY_SCOPE_AGENT);
  if (w == 0) dma16(gw + lane * 16, smb + 131072);
  if (bn == 0) k1_body<3>(sm, x, W, ldw, ws, bm, bn, w, lane, tid);
  else if (bn < 8) k1_body<0>(sm, x, W, ldw, ws, bm, bn, w, lane, tid);
  else if (bn < 12) k1_body<1>(sm, x, W, ldw, ws, bm, bn, w, lane, tid);
  else k1_body<2>(sm, x, W, ldw, ws, bm, bn, w, lane, tid);
  if (side) {
#pragma unroll
    for (int n = 0; n < 8; ++n) {
      u32x4 o;
#pragma unroll
      for (int dd = 0; dd < 4; ++dd) o[dd] = __builtin_amdgcn_perm(in[2 * dd + 1][n >> 1], in[2 * dd][n >> 1], (n & 1) ? 0x07060302u : 0x05040100u);
      *(u32x4*)(d + n * 16) = o;
    }
  }
}

DI void lds_add(LAS unsigned* p) { __hip_atomic_fetch_add(p, 1u, __ATOMIC_RELAXED, __HIP_MEMORY_SCOPE_WORKGROUP); }
DI void lds_wait(LAS unsigned* p, unsigned want) {
  while (__builtin_amdgcn_readfirstlane(__hip_atomic_load(p, __ATOMIC_RELAXED, __HIP_MEMORY_SCOPE_WORKGROUP)) < want) __builtin_amdgcn_s_sleep(1);
  asm volatile("" ::: "memory");
}
DI void lds_arrive(LAS unsigned* p, int lane) {
  WAIT_LGKM();
  if (lane == 0) lds_add(p);
  asm volatile("" ::: "memory");
}

template <bool BAR>
DI f32x16 gemm64(rsrc_t rs, unsigned A, unsigned B, char* red, LAS unsigned* cnt, int w, int lane) {
  const unsigned a0 = A + w * 8192 + (w & 1) * 32768, a1 = A + w * 8192 + ((w & 1) ^ 1) * 32768;
  const unsigned b0 = B + w * 8192 + (w >> 1) * 32768, b1 = B + w * 8192 + ((w >> 1) ^ 1) * 32768;
  f32x16 acc[4];
#pragma unroll
  for (int t = 0; t < 4; ++t)
#pragma unroll
    for (int i = 0; i < 16; ++i) acc[t][i] = 0.f;
  u32x4 af[8][2], bf[8][2];
#pragma unroll
  for (int k = 0; k < 8; ++k) {
    if (!BAR && k == 5) __builtin_amdgcn_sched_barrier(0);
    if (!BAR && k >= 5) {
#pragma unroll
      for (int t = 0; t < 4; ++t) acc[t] = MFMA(af[k - 5][t & 1], bf[k - 5][t >> 1], acc[t]);
      __builtin_amdgcn_sched_barrier(0);
    }
    af[k][0] = bld(a0 + k * 1024); af[k][1] = bld(a1 + k * 1024);
    bf[k][0] = bld(b0 + k * 1024); bf[k][1] = bld(b1 + k * 1024);
  }
#pragma unroll
  for (int k = BAR ? 0 : 3; k < 8; ++k)
#pragma unroll
    for (int t = 0; t < 4; ++t) acc[t] = MFMA(af[k][t & 1], bf[k][t >> 1], acc[t]);
#pragma unroll
  for (int t = 1; t < 4; ++t) {
    char* p = red + ((w ^ t) * 3 + t - 1) * 4096 + lane * 16;
#pragma unroll
    for (int g = 0; g < 4; ++g) *(f32x4*)(p + g * 1024) = f32x4{acc[t][4 * g], acc[t][4 * g + 1], acc[t][4 * g + 2], acc[t][4 * g + 3]};
  }
  f32x16 o = acc[0];
  if (BAR) { __syncthreads(); } else { lds_arrive(cnt, lane); lds_wait(cnt, 4); }
#pragma unroll
  for (int dlt = 0; dlt < 3; ++dlt)
#pragma unroll
    for (int g = 0; g < 4; ++g) {
      const f32x4 v = *(const f32x4*)(red + (w * 3 + dlt) * 4096 + lane * 16 + g * 1024);
#pragma unroll
      for (int e = 0; e < 4; ++e) o[4 * g + e] += v[e];
    }
  return o;
}

constexpr int TS = 2064, SLOT = 8 * TS;
constexpr int L_X1 = 4 * SLOT, L_H3 = L_X1 + 65536, L_CNT = L_H3 + 28672, K2_LDS = L_CNT + 64;

DI void attn_tile(const f32x16& s, const u32x4& v0, const u32x4& v1, f32x16& o) {
  f32x16 p;
#pragma unroll
  for (int i = 0; i < 16; ++i) p[i] = __builtin_amdgcn_exp2f(s[i]);
  o = MFMA(v0, pack8(p, 0, 1.0f), o);
  o = MFMA(v1, pack8(p, 8, 1.0f), o);
}

DI void attn_wave(char* sm, char* ws, int head, int qb, int w, int lane) {
  const int r = lane & 31, h = lane >> 5;
  const unsigned smb = (unsigned)(size_t)(LAS char*)sm;
  LAS unsigned* cnt = (LAS unsigned*)(sm + L_CNT);
  const char* kvh = ws + O_KV + (size_t)head * 131072;
  const char* kvg = kvh + w * 2048 + lane * 16;
  const int qt = qb * 8 + w;
  const u32x4 qf = ldg16(ws + O_QF + (size_t)(head * 64 + qt) * 1024 + lane * 16);
  float km = ((const float*)(ws + O_KMAX))[head * 64 + lane];
  WAIT_VM(0);
  lds_arrive(cnt + 0, lane);
#pragma unroll
  for (int o = 32; o >= 1; o >>= 1) km = fmaxf(km, __shfl_xor(km, o));
  float q2 = dot2(qf[0], qf[0], dot2(qf[1], qf[1], dot2(qf[2], qf[2], dot2(qf[3], qf[3], 0.f))));
  q2 += xhalf(q2);
  const float nb = -sqrtf(q2 * km) * 1.01f;
  f32x16 negb, o;
#pragma unroll
  for (int i = 0; i < 16; ++i) { negb[i] = nb; o[i] = 0.f; }
  const int va0 = r == 16 ? 2048 : 1024 + h * 256 + (r & 15) * 16;
  const int va1 = r == 16 ? 2048 : 1536 + h * 256 + (r & 15) * 16;
  for (int c = 0; c < 8; ++c) {
    if (c > 0) { WAIT_VM(0); lds_arrive(cnt + c, lane); }
    lds_wait(cnt + (c > 0 ? c - 1 : 0), 8);
    if (c + 2 < 8) {
      const unsigned sb = smb + ((c + 2) & 3) * SLOT + w * TS;
      dma16(kvg + (c + 2) * 16384, sb);
      dma16(kvg + (c + 2) * 16384 + 1024, sb + 1024);
    }
    const char* sl = sm + (c & 3) * SLOT;
    f32x16 s = MFMA(*(const u32x4*)(sl + lane * 16), qf, negb);
#pragma unroll
    for (int t = 0; t < 8; ++t) {
      const u32x4 v0 = *(const u32x4*)(sl + t * TS + va0);
      const u32x4 v1 = *(const u32x4*)(sl + t * TS + va1);
      f32x16 sn = s;
      if (t < 7) sn = MFMA(*(const u32x4*)(sl + (t + 1) * TS + lane * 16), qf, negb);
      attn_tile(s, v0, v1, o);
      s = sn;
    }
  }
  float l = __shfl(o[8], r);
  if (__builtin_amdgcn_ballot_w64(!(l >= 8.0e-25f && l < 3.0e38f)) != 0ull) {
    f32x16 z;
#pragma unroll
    for (int i = 0; i < 16; ++i) z[i] = 0.f;
    float m = -3.0e38f;
    for (int t = 0; t < 64; ++t) {
      const f32x16 s = MFMA(ldg16(kvh + t * 2048 + lane * 16), qf, z);
#pragma unroll
      for (int i = 0; i < 16; ++i) m = fmaxf(m, s[i]);
    }
    m = fmaxf(m, xhalf(m));
#pragma unroll
    for (int i = 0; i < 16; ++i) { negb[i] = -m; o[i] = 0.f; }
    const u32x4 ones = {0x3f803f80u, 0x3f803f80u, 0x3f803f80u, 0x3f803f80u};
    for (int t = 0; t < 64; ++t) {
      const f32x16 s = MFMA(ldg16(kvh + t * 2048 + lane * 16), qf, negb);
      u32x4 v0 = ldg16(kvh + t * 2048 + 1024 + h * 256 + (r & 15) * 16);
      u32x4 v1 = ldg16(kvh + t * 2048 + 1536 + h * 256 + (r & 15) * 16);
      if (r == 16) { v0 = ones; v1 = ones; }
      attn_tile(s, v0, v1, o);
    }
    l = __shfl(o[8], r);
  }
  *(u32x4*)(ws + O_OF + (size_t)(qt * 32 + head) * 1024 + lane * 16) = pack8(o, 0, 1.0f / l);
}

constexpr int D = 6;
template <int NFT>
DI void phase1(char* sm, rsrc_t rs, unsigned w3p, unsigned w4p, u32x4 (&ring)[D][2], int w, int lane) {
  f32x16 acc[NFT][2];
#pragma unroll
  for (int t = 0; t < 2 * NFT; ++t)
#pragma unroll
    for (int i = 0; i < 16; ++i) acc[t >> 1][t & 1][i] = 0.f;
  const char* xb = sm + L_X1 + lane * 16;
  u32x4 n0 = *(const u32x4*)xb, n1 = *(const u32x4*)(xb + 32768);
#pragma unroll
  for (int k = 0; k < 32; ++k) {
    const u32x4 b0 = n0, b1 = n1;
    if (k < 31) { n0 = *(const u32x4*)(xb + (k + 1) * 1024); n1 = *(const u32x4*)(xb + 32768 + (k + 1) * 1024); }
    const u32x4 a0 = ring[k % D][0], a1 = ring[k % D][1];
    acc[0][0] = MFMA(a0, b0, acc[0][0]);
    acc[0][1] = MFMA(a0, b1, acc[0][1]);
    if (NFT == 2) { acc[NFT - 1][0] = MFMA(a1, b0, acc[NFT - 1][0]); acc[NFT - 1][1] = MFMA(a1, b1, acc[NFT - 1][1]); }
    if (k + D < 32) { ring[k % D][0] = bld(w3p + (k + D) * 1024); if (NFT == 2) ring[k % D][1] = bld(w3p + 32768 + (k + D) * 1024); }
    else { ring[k % D][0] = bld(w4p + (k % D) * 1024); ring[k % D][1] = bld(w4p + 112 * 1024 + (k % D) * 1024); }
    __builtin_amdgcn_sched_barrier(0);
  }
#pragma unroll
  for (int f = 0; f < NFT; ++f)
#pragma unroll
    for (int rt = 0; rt < 2; ++rt) {
      f32x16 o;
#pragma unroll
      for (int i = 0; i < 16; ++i) o[i] = gelu(acc[f][rt][i]);
      char* p = sm + L_H3 + (rt * 14 + (2 * w + f) * 2) * 1024 + lane * 16;
      *(u32x4*)p = pack8(o, 0, 1.0f);
      *(u32x4*)(p + 1024) = pack8(o, 8, 1.0f);
    }
}

DI void mlp_wave(char* sm, const char* x, char* ws, int rb, int j, int w, int lane) {
  const int r = lane & 31, h = lane >> 5;
  LAS unsigned* cnt = (LAS unsigned*)(sm + L_CNT);
  const rsrc_t rs = ws_rsrc(ws);
  {
    const int nt = w & 1, rt = w >> 1;
    const char* xp = x + (size_t)(rb * 64 + rt * 32 + r) * 1024 + (j * 64 + nt * 32 + 4 * h) * 2;
    u32x2 xv[4];
#pragma unroll
    for (int g = 0; g < 4; ++g) xv[g] = *(const u32x2*)(xp + g * 16);
    f32x16 o = gemm64<false>(rs, O_W2 + j * 65536, O_HS + rb * 65536, sm + L_X1, cnt + 8, w, lane);
    float s1 = 0.f, s2 = 0.f;
#pragma unroll
    for (int g = 0; g < 4; ++g) {
      o[4 * g] += lo16(xv[g][0]); o[4 * g + 1] += hi16(xv[g][0]); o[4 * g + 2] += lo16(xv[g][1]); o[4 * g + 3] += hi16(xv[g][1]);
    }
    const u32x4 p0 = pack8(o, 0, 1.0f), p1 = pack8(o, 8, 1.0f);
#pragma unroll
    for (int d = 0; d < 4; ++d) {
      s1 = dot2(p0[d], 0x3f803f80u, s1); s2 = dot2(p0[d], p0[d], s2);
      s1 = dot2(p1[d], 0x3f803f80u, s1); s2 = dot2(p1[d], p1[d], s2);
    }
    s1 += xhalf(s1); s2 += xhalf(s2);
    const unsigned fo = O_X1 + ((rb * 2 + rt) * 32 + j * 4 + nt * 2) * 1024;
    __builtin_amdgcn_raw_buffer_store_b128(p0, rs, lane * 16, fo, 16);
    __builtin_amdgcn_raw_buffer_store_b128(p1, rs, lane * 16, fo + 1024, 16);
    if (h == 0) {
      const u32x2 st = {__float_as_uint(s1), __float_as_uint(s2)};
      __builtin_amdgcn_raw_buffer_store_b64(st, rs, r * 8, O_STAT + ((rb * 2 + rt) * 16 + j * 2 + nt) * 256, 16);
    }
    WAIT_VM(0);
    if (lane == 0) __hip_atomic_fetch_add((unsigned*)(ws + O_CNT + rb * 256), 1u, __ATOMIC_RELAXED, __HIP_MEMORY_SCOPE_AGENT);
  }
  const unsigned w3p = O_W3 + (j * 7 + 2 * w) * 32768;
  const int f1 = w < 3 ? 32768 : 0;
  u32x4 ring[D][2];
#pragma unroll
  for (int k = 0; k < D; ++k) { ring[k][0] = bld(w3p + k * 1024); ring[k][1] = bld(w3p + f1 + k * 1024); }
  {
    unsigned* gc = (unsigned*)(ws + O_CNT + rb * 256);
    while (__builtin_amdgcn_readfirstlane(__hip_atomic_load(gc, __ATOMIC_RELAXED, __HIP_MEMORY_SCOPE_AGENT)) < 32u) __builtin_amdgcn_s_sleep(2);
    asm volatile("" ::: "memory");
    const int rt = w >> 1, sb = (w & 1) * 16;
    float s1 = 0.f, s2 = 0.f;
    u32x2 st[8];
#pragma unroll
    for (int k = 0; k < 8; ++k)
      st[k] = __builtin_amdgcn_raw_buffer_load_b64(rs, h * 2048 + r * 8, O_STAT + ((rb * 2 + rt) * 16 + k) * 256, 16);
    const unsigned fo = O_X1 + ((rb * 2 + rt) * 32 + sb) * 1024;
    char* dst = sm + L_X1 + (rt * 32 + sb) * 1024 + lane * 16;
    u32x4 xf[8];
#pragma unroll
    for (int k = 0; k < 8; ++k) xf[k] = __builtin_amdgcn_raw_buffer_load_b128(rs, lane * 16, fo + k * 1024, 16);
#pragma unroll
    for (int k = 0; k < 8; ++k) { s1 += __uint_as_float(st[k][0]); s2 += __uint_as_float(st[k][1]); }
    s1 += xhalf(s1); s2 += xhalf(s2);
    const float mean = s1 * (1.0f / 512.0f);
    const float rstd = rsqrtf(fmaxf(s2 * (1.0f / 512.0f) - mean * mean, 0.f) + 1e-5f), nm = -mean * rstd;
#pragma unroll
    for (int hf = 0; hf < 2; ++hf) {
#pragma unroll
      for (int k = 0; k < 8; ++k) {
        u32x4 o;
#pragma unroll
        for (int d = 0; d < 4; ++d) o[d] = pk2(__builtin_fmaf(lo16(xf[k][d]), rstd, nm), __builtin_fmaf(hi16(xf[k][d]), rstd, nm));
        if (hf == 0) xf[k] = __builtin_amdgcn_raw_buffer_load_b128(rs, lane * 16, fo + (8 + k) * 1024, 16);
        *(u32x4*)(dst + (hf * 8 + k) * 1024) = o;
      }
    }
    lds_arrive(cnt + 9, lane);
    lds_wait(cnt + 9, 4);
  }
  f32x16 acc[2][2];
  const unsigned w4p = O_W4 + (4 * w * 112 + j * 14) * 1024;
  {
    if (w < 3) phase1<2>(sm, rs, w3p, w4p, ring, w, lane); else phase1<1>(sm, rs, w3p, w4p, ring, w, lane);
    lds_arrive(cnt + 10, lane);
    lds_wait(cnt + 10, 4);
  }
  {
    const char* hb = sm + L_H3 + lane * 16;
    u32x4 n0 = *(const u32x4*)hb, n1 = *(const u32x4*)(hb + 14336);
#pragma unroll
    for (int ps = 0; ps < 2; ++ps) {
#pragma unroll
      for (int t = 0; t < 4; ++t)
#pragma unroll
        for (int i = 0; i < 16; ++i) acc[t >> 1][t & 1][i] = 0.f;
#pragma unroll
      for (int k = 0; k < 14; ++k) {
        const int kk = ps * 14 + k, kn = kk + D;
        const u32x4 b0 = n0, b1 = n1;
        if (kk < 27) { n0 = *(const u32x4*)(hb + ((k + 1) % 14) * 1024); n1 = *(const u32x4*)(hb + 14336 + ((k + 1) % 14) * 1024); }
        const u32x4 a0 = ring[kk % D][0], a1 = ring[kk % D][1];
        acc[0][0] = MFMA(a0, b0, acc[0][0]);
        acc[0][1] = MFMA(a0, b1, acc[0][1]);
        acc[1][0] = MFMA(a1, b0, acc[1][0]);
        acc[1][1] = MFMA(a1, b1, acc[1][1]);
        if (kn < 28) {
          const unsigned p = w4p + ((kn / 14) * 224 + kn % 14) * 1024;
          ring[kk % D][0] = bld(p); ring[kk % D][1] = bld(p + 112 * 1024);
        }
        __builtin_amdgcn_sched_barrier(0);
      }
#pragma unroll
      for (int nt = 0; nt < 2; ++nt)
#pragma unroll
        for (int rt = 0; rt < 2; ++rt) {
          const unsigned p = O_PART + j * 2 * MB + ((rb * 2 + rt) * 32 + (4 * w + 2 * ps + nt) * 2) * 1024;
          __builtin_amdgcn_raw_buffer_store_b128(pack8(acc[nt][rt], 0, 1.0f), rs, lane * 16, p, 0);
          __builtin_amdgcn_raw_buffer_store_b128(pack8(acc[nt][rt], 8, 1.0f), rs, lane * 16, p + 1024, 0);
        }
      __builtin_amdgcn_sched_barrier(0);
    }
  }
}

__global__ __launch_bounds__(768, 3) void k2(const char* __restrict__ x, char* __restrict__ ws) {
  extern __shared__ __attribute__((aligned(16))) char sm[];
  const int tid = threadIdx.x, lane = tid & 63, w = __builtin_amdgcn_readfirstlane(tid >> 6);
  const int b = blockIdx.x, xcd = b & 7, i = b >> 3;
  const int head = xcd * 4 + (i >> 3), qb = i & 7;
  if (w < 8) {
    const unsigned smb = (unsigned)(size_t)(LAS char*)sm;
    const char* kvg = ws + O_KV + (size_t)head * 131072 + w * 2048 + lane * 16;
#pragma unroll
    for (int c = 0; c < 2; ++c) {
      dma16(kvg + c * 16384, smb + c * SLOT + w * TS);
      dma16(kvg + c * 16384 + 1024, smb + c * SLOT + w * TS + 1024);
    }
    if (lane < 4) *(u32x4*)(sm + lane * SLOT + w * TS + 2048) = u32x4{0x3f803f80u, 0x3f803f80u, 0x3f803f80u, 0x3f803f80u};
  } else if (w == 8 && lane < 16) {
    *(unsigned*)(sm + L_CNT + lane * 4) = 0u;
  }
  WAIT_LGKM();
  __builtin_amdgcn_s_barrier();
  asm volatile("" ::: "memory");
  if (w < 8) attn_wave(sm, ws, head, qb, w, lane);
  else { __builtin_amdgcn_s_setprio(3); mlp_wave(sm, x, ws, i, xcd, w - 8, lane); }
}

__global__ __launch_bounds__(256, 1) void k3(const char* __restrict__ ws, char* __restrict__ out) {
  __shared__ __attribute__((aligned(16))) char red[49152];
  const int tid = threadIdx.x, lane = tid & 63, w = __builtin_amdgcn_readfirstlane(tid >> 6);
  const int b = blockIdx.x, j = b & 7, rb = b >> 3;
  const int r = lane & 31, h = lane >> 5, nt = w & 1, rt = w >> 1;
  const unsigned fo = ((rb * 2 + rt) * 32 + j * 4 + nt * 2) * 1024;
  const rsrc_t rs = ws_rsrc(ws);
  u32x4 pp[9][2];
#pragma unroll
  for (int s = 0; s < 9; ++s) {
    const unsigned p = (s < 8 ? O_PART + s * 2 * MB : O_X1) + fo;
    pp[s][0] = bld(p);
    pp[s][1] = bld(p + 1024);
  }
  const float g = ((const float*)(ws + O_G))[rb * 64 + rt * 32 + r];
  const f32x16 a = gemm64<true>(rs, O_WO + j * 65536, O_OF + rb * 65536, red, nullptr, w, lane);
  f32x16 o;
#pragma unroll
  for (int i = 0; i < 16; ++i) o[i] = 0.f;
#pragma unroll
  for (int s = 0; s < 9; ++s)
#pragma unroll
    for (int f = 0; f < 2; ++f)
#pragma unroll
      for (int d = 0; d < 4; ++d) { o[8 * f + 2 * d] += lo16(pp[s][f][d]); o[8 * f + 2 * d + 1] += hi16(pp[s][f][d]); }
  char* op = out + (size_t)(rb * 64 + rt * 32 + r) * 1024 + (j * 64 + nt * 32 + 4 * h) * 2;
#pragma unroll
  for (int q = 0; q < 4; ++q) {
    float v[4];
#pragma unroll
    for (int e = 0; e < 4; ++e) v[e] = g * a[4 * q + e] + (1.0f - g) * o[4 * q + e];
    *(u32x2*)(op + q * 16) = u32x2{pk2(v[0], v[1]), pk2(v[2], v[3])};
  }
}

extern "C" void kernel_launch(void* const* d_in, const int* in_sizes, int n_in, void* d_out, int out_size, void* d_ws, size_t ws_size,
                              hipStream_t stream) {
  (void)hipFuncSetAttribute((const void*)k1, hipFuncAttributeMaxDynamicSharedMemorySize, K1_LDS);
  (void)hipFuncSetAttribute((const void*)k2, hipFuncAttributeMaxDynamicSharedMemorySize, K2_LDS);
  char* ws = (char*)d_ws;
  k1<<<256, 256, K1_LDS, stream>>>((const char*)d_in[0], (const char*)d_in[1], (const char*)d_in[4], (const char*)d_in[2], (const char*)d_in[5],
                                   (const char*)d_in[6], (const char*)d_in[7], (const char*)d_in[8], ws);
  k2<<<256, 768, K2_LDS, stream>>>((const char*)d_in[0], ws);
  k3<<<256, 256, 0, stream>>>(ws, (char*)d_out);
}
```

```cpp
#include <hip/hip_runtime.h>
#include <hip/hip_bf16.h>

typedef __attribute__((ext_vector_type(8))) __bf16 bf16x8;
typedef __attribute__((ext_vector_type(2))) __bf16 bf16x2;
typedef __attribute__((ext_vector_type(16))) float f32x16;
typedef __attribute__((ext_vector_type(4))) float f32x4;
typedef __attribute__((ext_vector_type(2))) float f32x2;
typedef __attribute__((ext_vector_type(4))) unsigned u32x4;
typedef __attribute__((ext_vector_type(2))) unsigned u32x2;
typedef __attribute__((ext_vector_type(4))) short s16x4;
#define DI __device__ __forceinline__
#define LAS __attribute__((address_space(3)))
#define MFMA(a, b, c) __builtin_amdgcn_mfma_f32_32x32x16_bf16(__builtin_bit_cast(bf16x8, a), __builtin_bit_cast(bf16x8, b), c, 0, 0, 0)

constexpr unsigned MB = 1u << 20;
constexpr unsigned O_QF = 0, O_KV = 2 * MB, O_HS = 6 * MB, O_W2 = 8 * MB, O_WO = 8 * MB + MB / 2, O_W3 = 9 * MB, O_W4 = 11 * MB,
                 O_X1 = 13 * MB, O_OF = 15 * MB, O_PART = 17 * MB, O_STAT = 33 * MB, O_KMAX = 34 * MB, O_G = 34 * MB + 16384,
                 O_CNT = 34 * MB + 32768;

DI unsigned pk2(float a, float b) { f32x2 v = {a, b}; return __builtin_bit_cast(unsigned, __builtin_convertvector(v, bf16x2)); }
DI float lo16(unsigned u) { return __uint_as_float(u << 16); }
DI float hi16(unsigned u) { return __uint_as_float(u & 0xffff0000u); }
DI float dot2(unsigned a, unsigned b, float c) { return __builtin_amdgcn_fdot2_f32_bf16(__builtin_bit_cast(bf16x2, a), __builtin_bit_cast(bf16x2, b), c, false); }
DI float xhalf(float v) { return __shfl_xor(v, 32); }
DI float gelu(float x) {
  float z = x * __builtin_fmaf(x * x, -0.10294324f, -2.3022082f);
  return x * __builtin_amdgcn_rcpf(1.0f + __builtin_amdgcn_exp2f(z));
}
DI void dma16(const void* g, unsigned lds_base) {
  unsigned sv;
  asm volatile("s_mov_b32 %0, m0\n\ts_mov_b32 m0, %2\n\ts_nop 0\n\tglobal_load_lds_dwordx4 %1, off\n\ts_mov_b32 m0, %0"
               : "=&s"(sv) : "v"(g), "s"(lds_base) : "memory");
}
#define WAIT_VM(n) asm volatile("s_waitcnt vmcnt(" #n ")" ::: "memory")
#define WAIT_LGKM() asm volatile("s_waitcnt lgkmcnt(0)" ::: "memory")
DI u32x4 pack8(const f32x16& a, int o, float m) {
  u32x4 r = {pk2(a[o] * m, a[o + 1] * m), pk2(a[o + 2] * m, a[o + 3] * m), pk2(a[o + 4] * m, a[o + 5] * m), pk2(a[o + 6] * m, a[o + 7] * m)};
  return r;
}
#define TS_(kid, k)
DI u32x4 ldg16(const char* p) { return *(const u32x4*)p; }
typedef __amdgpu_buffer_rsrc_t rsrc_t;
DI rsrc_t ws_rsrc(const char* ws) { return __builtin_amdgcn_make_buffer_rsrc((void*)ws, (short)0, (int)(35 * MB), 0x00020000); }
#define bld(soff) __builtin_amdgcn_raw_buffer_load_b128(rs, lane * 16, (unsigned)(soff), 0)

constexpr int K1_LDS = 4 * 32768 + 1024 + 1024;

template <int ROLE>
DI void k1_body(char* sm, const char* x, const char* W, int ldw, char* ws, int bm, int bn, int w, int lane, int tid) {
  const int wn = w & 1, wm = w >> 1, r = lane & 31, h = lane >> 5;
  const unsigned smb = (unsigned)(size_t)(LAS char*)sm;
  const int xr = w * 32 + (lane >> 3);
  const char* xg = x + (size_t)(bm * 128 + xr) * 1024;
  const int wr = w * 16 + (lane >> 4);
  const char* wg = W + (size_t)wr * ldw;
  const int rot = (bm + bn) & 7;
  auto issue = [&](int c) {
    const unsigned sb = smb + (c & 3) * 32768 + w * 4096;
    const int cc = (c + rot) & 7;
#pragma unroll
    for (int p = 0; p < 4; ++p) {
      const int row = xr + 8 * p;
      dma16(xg + p * 8192 + cc * 128 + (((lane & 7) ^ ((row >> 1) & 7)) << 4), sb + p * 1024);
    }
#pragma unroll
    for (int p = 0; p < 4; ++p) {
      const int row = wr + 4 * p;
      dma16(wg + (size_t)(cc * 64 + 4 * p) * ldw + (((lane & 15) ^ (((row & 3) << 2) | ((row >> 2) & 3))) << 4), sb + 16384 + p * 1024);
    }
  };
  TS_(1, 0);
  issue(0);
  issue(1);
  issue(2);
  issue(3);
  f32x16 acc[2][2];
#pragma unroll
  for (int a = 0; a < 2; ++a)
#pragma unroll
    for (int b = 0; b < 2; ++b)
#pragma unroll
      for (int i = 0; i < 16; ++i) acc[a][b][i] = 0.f;
  float s1[4] = {0.f, 0.f, 0.f, 0.f}, s2[4] = {0.f, 0.f, 0.f, 0.f}, cs[4] = {0.f, 0.f, 0.f, 0.f};
  const int xsw = (r >> 1) & 7;
  const int blk = (lane >> 4) & 1, q = (lane & 15) >> 2, pp = lane & 3;
  auto rd = [&](int c, int ks, u32x4 (&xf)[2], u32x4 (&wf)[2], u32x4& gwv) {
    const char* xs = sm + (c & 3) * 32768;
    const char* wsm = xs + 16384;
#pragma unroll
    for (int b = 0; b < 2; ++b) xf[b] = *(const u32x4*)(xs + (wm * 64 + (b ^ wn) * 32 + r) * 128 + (((2 * ks + h) ^ xsw) << 4));
#pragma unroll
    for (int a = 0; a < 2; ++a)
#pragma unroll
      for (int t = 0; t < 2; ++t) {
        const int row = 16 * ks + 8 * h + 4 * t + q;
        const int ch = 4 * (wn * 2 + (a ^ wm)) + 2 * blk + (pp >> 1);
        const int off = 256 * row + 16 * (ch ^ (((row & 3) << 2) | ((row >> 2) & 3))) + 8 * (pp & 1);
        u32x2 v = __builtin_bit_cast(u32x2, __builtin_amdgcn_ds_read_tr16_b64_v4i16((LAS s16x4*)(wsm + off)));
        wf[a][2 * t] = v[0];
        wf[a][2 * t + 1] = v[1];
      }
    if (ROLE == 3) gwv = *(const u32x4*)(sm + 131072 + ((c + rot) & 7) * 128 + ks * 32 + h * 16);
  };
  u32x4 xn[2], wnx[2], gn = {0u, 0u, 0u, 0u};
  WAIT_VM(24);
  __builtin_amdgcn_s_barrier();
  asm volatile("" ::: "memory");
  rd(0, 0, xn, wnx, gn);
  for (int c = 0; c < 8; ++c) {
    TS_(1, 1 + c);
#pragma unroll
    for (int ks = 0; ks < 4; ++ks) {
      const u32x4 xf[2] = {xn[0], xn[1]}, wf[2] = {wnx[0], wnx[1]}, gw = gn;
      if (ks < 3) {
        rd(c, ks + 1, xn, wnx, gn);
      } else if (c < 7) {
        WAIT_LGKM();
        if (c < 5) WAIT_VM(16); else if (c == 5) WAIT_VM(8); else WAIT_VM(0);
        __builtin_amdgcn_s_barrier();
        asm volatile("" ::: "memory");
        rd(c + 1, 0, xn, wnx, gn);
        if (c < 4) issue(c + 4);
      }
      __builtin_amdgcn_sched_barrier(0);
#pragma unroll
      for (int a = 0; a < 2; ++a)
#pragma unroll
        for (int b = 0; b < 2; ++b) acc[a][b] = ROLE == 1 ? MFMA(xf[b], wf[a], acc[a][b]) : MFMA(wf[a], xf[b], acc[a][b]);
      if (ROLE == 2) {
#pragma unroll
        for (int d = 0; d < 4; ++d) {
          s1[d] = dot2(xf[0][d], 0x3f803f80u, s1[d]);
          s2[d] = dot2(xf[0][d], xf[0][d], s2[d]);
          cs[d] = dot2(wf[0][d], 0x3f803f80u, cs[d]);
        }
#pragma unroll
        for (int m = 0; m < 4; ++m) { __builtin_amdgcn_sched_group_barrier(0x008, 1, 0); __builtin_amdgcn_sched_group_barrier(0x002, 3, 0); }
      }
      if (ROLE == 3) {
#pragma unroll
        for (int d = 0; d < 4; ++d) s1[d] = dot2(xf[0][d], gw[d], s1[d]);
#pragma unroll
        for (int m = 0; m < 4; ++m) { __builtin_amdgcn_sched_group_barrier(0x008, 1, 0); __builtin_amdgcn_sched_group_barrier(0x002, 1, 0); }
      }
      __builtin_amdgcn_sched_barrier(0);
    }
  }
  TS_(1, 9);
  const int tile0 = bm * 4 + wm * 2;
  if (ROLE == 0 || ROLE == 3) {
    const bool isq = bn < 4;
    const float m = isq ? 0.25f * 1.4426950409f : 1.0f;
    const int head0 = (bn & 3) * 8 + wn * 4;
#pragma unroll
    for (int nt = 0; nt < 2; ++nt)
#pragma unroll
      for (int rt = 0; rt < 2; ++rt)
#pragma unroll
        for (int s = 0; s < 2; ++s) {
          const int head = head0 + (nt ^ wm) * 2 + s;
          const int tile = tile0 + (rt ^ wn);
          const u32x4 v = pack8(acc[nt][rt], 8 * s, m);
          if (isq) {
            *(u32x4*)(ws + O_QF + (size_t)(head * 64 + tile) * 1024 + lane * 16) = v;
          } else {
            *(u32x4*)(ws + O_KV + (size_t)(head * 64 + tile) * 2048 + lane * 16) = v;
            float n2 = dot2(v[0], v[0], dot2(v[1], v[1], dot2(v[2], v[2], dot2(v[3], v[3], 0.f))));
            n2 += xhalf(n2);
#pragma unroll
            for (int o = 16; o >= 1; o >>= 1) n2 = fmaxf(n2, __shfl_xor(n2, o));
            if (lane == 0) ((float*)(ws + O_KMAX))[head * 64 + tile] = n2;
          }
        }
    if (ROLE == 3) {
      float z = (s1[0] + s1[1]) + (s1[2] + s1[3]);
      z += xhalf(z);
      if (h == 0) ((float*)(ws + O_G))[(tile0 + wn) * 32 + r] = 1.0f / (1.0f + __expf(-z));
    }
  } else if (ROLE == 1) {
#pragma unroll
    for (int nt = 0; nt < 2; ++nt)
#pragma unroll
      for (int rt = 0; rt < 2; ++rt)
#pragma unroll
        for (int s = 0; s < 2; ++s) {
          const int head = (bn & 3) * 8 + wn * 4 + (nt ^ wm) * 2 + (r >> 4);
          *(u32x4*)(ws + O_KV + (size_t)(head * 64 + tile0 + (rt ^ wn)) * 2048 + 1024 + s * 512 + h * 256 + (r & 15) * 16) = pack8(acc[nt][rt], 8 * s, 1.0f);
        }
  } else {
    float* cl = (float*)(sm + 131072);
    {
      float t = (cs[0] + cs[1]) + (cs[2] + cs[3]), u1 = (s1[0] + s1[1]) + (s1[2] + s1[3]), u2 = (s2[0] + s2[1]) + (s2[2] + s2[3]);
      t += xhalf(t); u1 += xhalf(u1); u2 += xhalf(u2);
      if (h == 0) { cl[wn * 64 + wm * 32 + r] = t; cl[128 + wm * 64 + wn * 32 + r] = u1; cl[256 + wm * 64 + wn * 32 + r] = u2; }
      WAIT_LGKM();
      __builtin_amdgcn_s_barrier();
      asm volatile("" ::: "memory");
    }
    const int g = bn & 3;
    const int sh = (g & 1) ? -(1 + (g >> 1)) : (1 + (g >> 1));
#pragma unroll
    for (int rt = 0; rt < 2; ++rt) {
      const float mean = cl[128 + wm * 64 + (rt ^ wn) * 32 + r] * (1.0f / 512.0f);
      const float var = fmaxf(cl[256 + wm * 64 + (rt ^ wn) * 32 + r] * (1.0f / 512.0f) - mean * mean, 0.f);
      const float rstd = rsqrtf(var + 1e-5f);
      const float nm = -mean * rstd;
      const int rd = (tile0 + (rt ^ wn)) * 32 + r + sh;
      const bool ok = rd >= 0 && rd < 2048;
      const int rw = rd & 2047;
#pragma unroll
      for (int nt = 0; nt < 2; ++nt) {
        f32x16 o;
#pragma unroll
        for (int gq = 0; gq < 4; ++gq) {
          const f32x4 c4 = *(const f32x4*)(cl + wn * 64 + (nt ^ wm) * 32 + 8 * gq + 4 * h);
#pragma unroll
          for (int e = 0; e < 4; ++e) o[4 * gq + e] = gelu(__builtin_fmaf(acc[nt][rt][4 * gq + e], rstd, nm * c4[e]));
        }
#pragma unroll
        for (int s = 0; s < 2; ++s) {
          u32x4 v = pack8(o, 8 * s, 1.0f);
          if (!ok) v = u32x4{0u, 0u, 0u, 0u};
          *(u32x4*)(ws + O_HS + (size_t)((rw >> 5) * 32 + g * 8 + wn * 4 + (nt ^ wm) * 2 + s) * 1024 + ((rw & 31) + 32 * h) * 16) = v;
        }
      }
    }
  }
}

__global__ __launch_bounds__(256, 1) void k1(const char* __restrict__ x, const char* __restrict__ wqkv, const char* __restrict__ w1,
                                             const char* __restrict__ wo, const char* __restrict__ w2, const char* __restrict__ w3,
                                             const char* __restrict__ w4, const char* __restrict__ gw, char* __restrict__ ws) {
  extern __shared__ __attribute__((aligned(16))) char sm[];
  const int tid = threadIdx.x, lane = tid & 63, w = __builtin_amdgcn_readfirstlane(tid >> 6);
  const int b = blockIdx.x, xcd = b & 7, i = b >> 3;
  const int bm = (xcd >> 1) * 4 + (i & 3), bn = (xcd & 1) * 8 + (i >> 2);
  const char* W = bn < 12 ? wqkv + bn * 256 : w1 + (bn - 12) * 256;
  const int ldw = bn < 12 ? 3072 : 1024;
  const unsigned smb = (unsigned)(size_t)(LAS char*)sm;
  const int u = (b & 7) * 128 + (b >> 3) * 4 + w;
  const int uu = ((u * 9) >> 4);
  const bool side = (((u + 1) * 9) >> 4) != uu;
  u32x4 in[8];
  char* d = ws;
  if (side) {
    const char* src; char* dst; int ld, ks, nb, nks;
    if (uu < 64) { src = w2; dst = ws + O_W2; ld = 1024; ks = uu >> 1; nb = uu & 1; nks = 32; }
    else if (uu < 128) { src = wo; dst = ws + O_WO; ld = 1024; ks = (uu - 64) >> 1; nb = uu & 1; nks = 32; }
    else if (uu < 352) { src = w3; dst = ws + O_W3; ld = 3584; ks = (uu - 128) / 7; nb = (uu - 128) % 7; nks = 32; }
    else { src = w4; dst = ws + O_W4; ld = 1024; ks = (uu - 352) >> 1; nb = uu & 1; nks = 112; }
    const int r = lane & 31, h = lane >> 5;
#pragma unroll
    for (int j = 0; j < 8; ++j) in[j] = ldg16(src + (size_t)(ks * 16 + 8 * (j >> 2) + 4 * h + (j & 3)) * ld + nb * 512 + r * 16);
    d = dst + (size_t)((nb * 8 + (r >> 2)) * nks + ks) * 1024 + ((r & 3) * 8 + 32 * h) * 16;
  } else {
#pragma unroll
    for (int j = 0; j < 8; ++j) in[j] = u32x4{0u, 0u, 0u, 0u};
  }
  if (b == 0 && tid < 32) __hip_atomic_store((unsigned*)(ws + O_CNT + tid * 256), 0u, __ATOMIC_RELAXED, __HIP_MEMORY_SCOPE_AGENT);
  if (w == 0 && bn == 0) dma16(gw + lane * 16, smb + 131072);
  if (bn == 0) k1_body<3>(sm, x, W, ldw, ws, bm, bn, w, lane, tid);
  else if (bn < 8) k1_body<0>(sm, x, W, ldw, ws, bm, bn, w, lane, tid);
  else if (bn < 12) k1_body<1>(sm, x, W, ldw, ws, bm, bn, w, lane, tid);
  else k1_body<2>(sm, x, W, ldw, ws, bm, bn, w, lane, tid);
  TS_(1, 10);
  if (side) {
#pragma unroll
    for (int n = 0; n < 8; ++n) {
      u32x4 o;
#pragma unroll
      for (int dd = 0; dd < 4; ++dd) o[dd] = __builtin_amdgcn_perm(in[2 * dd + 1][n >> 1], in[2 * dd][n >> 1], (n & 1) ? 0x07060302u : 0x05040100u);
      *(u32x4*)(d + n * 16) = o;
    }
  }
  TS_(1, 11);
}

DI void lds_add(LAS unsigned* p) { __hip_atomic_fetch_add(p, 1u, __ATOMIC_RELAXED, __HIP_MEMORY_SCOPE_WORKGROUP); }
DI void lds_wait(LAS unsigned* p, unsigned want) {
  while (__builtin_amdgcn_readfirstlane(__hip_atomic_load(p, __ATOMIC_RELAXED, __HIP_MEMORY_SCOPE_WORKGROUP)) < want) __builtin_amdgcn_s_sleep(1);
  asm volatile("" ::: "memory");
}
DI void lds_arrive(LAS unsigned* p, int lane) {
  WAIT_LGKM();
  if (lane == 0) lds_add(p);
  asm volatile("" ::: "memory");
}

template <bool BAR>
DI f32x16 gemm64(rsrc_t rs, unsigned A, unsigned B, char* red, LAS unsigned* cnt, int w, int lane) {
  const unsigned a0 = A + w * 8192 + (w & 1) * 32768, a1 = A + w * 8192 + ((w & 1) ^ 1) * 32768;
  const unsigned b0 = B + w * 8192 + (w >> 1) * 32768, b1 = B + w * 8192 + ((w >> 1) ^ 1) * 32768;
  f32x16 acc[4];
#pragma unroll
  for (int t = 0; t < 4; ++t)
#pragma unroll
    for (int i = 0; i < 16; ++i) acc[t][i] = 0.f;
  u32x4 af[8][2], bf[8][2];
#pragma unroll
  for (int k = 0; k < 8; ++k) {
    if (!BAR && k == 5) __builtin_amdgcn_sched_barrier(0);
    if (!BAR && k >= 5) {
#pragma unroll
      for (int t = 0; t < 4; ++t) acc[t] = MFMA(af[k - 5][t & 1], bf[k - 5][t >> 1], acc[t]);
      __builtin_amdgcn_sched_barrier(0);
    }
    af[k][0] = bld(a0 + k * 1024); af[k][1] = bld(a1 + k * 1024);
    bf[k][0] = bld(b0 + k * 1024); bf[k][1] = bld(b1 + k * 1024);
  }
#pragma unroll
  for (int k = BAR ? 0 : 3; k < 8; ++k)
#pragma unroll
    for (int t = 0; t < 4; ++t) acc[t] = MFMA(af[k][t & 1], bf[k][t >> 1], acc[t]);
#pragma unroll
  for (int t = 1; t < 4; ++t) {
    char* p = red + ((w ^ t) * 3 + t - 1) * 4096 + lane * 16;
#pragma unroll
    for (int g = 0; g < 4; ++g) *(f32x4*)(p + g * 1024) = f32x4{acc[t][4 * g], acc[t][4 * g + 1], acc[t][4 * g + 2], acc[t][4 * g + 3]};
  }
  f32x16 o = acc[0];
  if (BAR) { __syncthreads(); } else { lds_arrive(cnt, lane); lds_wait(cnt, 4); }
#pragma unroll
  for (int dlt = 0; dlt < 3; ++dlt)
#pragma unroll
    for (int g = 0; g < 4; ++g) {
      const f32x4 v = *(const f32x4*)(red + (w * 3 + dlt) * 4096 + lane * 16 + g * 1024);
#pragma unroll
      for (int e = 0; e < 4; ++e) o[4 * g + e] += v[e];
    }
  return o;
}

#define ATT_PRIO 1
#define MLP_PRIO 0
constexpr int TS = 2064, SLOT = 8 * TS;
constexpr int L_X1 = 4 * SLOT, L_H3 = L_X1 + 65536, L_CNT = L_H3 + 28672, K2_LDS = L_CNT + 64;

#define SB() __builtin_amdgcn_sched_barrier(0)
DI void attn_tile(const f32x16& s, const u32x4& v0, const u32x4& v1, f32x16& o) {
  f32x16 p;
#pragma unroll
  for (int i = 0; i < 8; ++i) p[i] = __builtin_amdgcn_exp2f(s[i]);
  const u32x4 p0 = pack8(p, 0, 1.0f);
  SB();
  o = MFMA(v0, p0, o);
  SB();
#pragma unroll
  for (int i = 8; i < 16; ++i) p[i] = __builtin_amdgcn_exp2f(s[i]);
  const u32x4 p1 = pack8(p, 8, 1.0f);
  SB();
  o = MFMA(v1, p1, o);
  SB();
}

DI void attn_wave(char* sm, char* ws, int head, int qb, int w, int lane, const u32x4 qf, float km) {
  const int r = lane & 31, h = lane >> 5;
  const unsigned smb = (unsigned)(size_t)(LAS char*)sm;
  LAS unsigned* cnt = (LAS unsigned*)(sm + L_CNT);
  const char* kvh = ws + O_KV + (size_t)head * 131072;
  const char* kvg = kvh + w * 2048 + lane * 16;
  const int qt = qb * 8 + w;
#pragma unroll
  for (int o = 32; o >= 1; o >>= 1) km = fmaxf(km, __shfl_xor(km, o));
  float q2 = dot2(qf[0], qf[0], dot2(qf[1], qf[1], dot2(qf[2], qf[2], dot2(qf[3], qf[3], 0.f))));
  q2 += xhalf(q2);
  const float nb = -sqrtf(q2 * km) * 1.01f;
  f32x16 negb, o;
#pragma unroll
  for (int i = 0; i < 16; ++i) { negb[i] = nb; o[i] = 0.f; }
  const int va0 = r == 16 ? 2048 : 1024 + h * 256 + (r & 15) * 16;
  const int va1 = r == 16 ? 2048 : 1536 + h * 256 + (r & 15) * 16;
  lds_wait(cnt + 11, 8);
  u32x4 kf = *(const u32x4*)(sm + TS + lane * 16), v0 = *(const u32x4*)(sm + va0), v1 = *(const u32x4*)(sm + va1);
  f32x16 s = MFMA(*(const u32x4*)(sm + lane * 16), qf, negb);
  for (int c = 0; c < 8; ++c) {
    WAIT_VM(0);
    lds_arrive(cnt + c, lane);
    if (c + 2 < 8) {
      const unsigned sb = smb + ((c + 2) & 3) * SLOT + w * TS;
      dma16(kvg + (c + 2) * 16384, sb);
      dma16(kvg + (c + 2) * 16384 + 1024, sb + 1024);
    }
    TS_(2, 1 + c);
    const char* sl = sm + (c & 3) * SLOT;
    const char* sx = sm + ((c + 1) & 3) * SLOT;
    unsigned fl = 0;
#pragma unroll
    for (int t = 0; t < 8; ++t) {
      if (t == 4) fl = __hip_atomic_load(cnt + c, __ATOMIC_RELAXED, __HIP_MEMORY_SCOPE_WORKGROUP);
      if (t == 6 && __builtin_amdgcn_readfirstlane(fl) < 8u) lds_wait(cnt + c, 8);
      const u32x4 kn = *(const u32x4*)((t < 6 ? sl + (t + 2) * TS : sx + (t - 6) * TS) + lane * 16);
      const u32x4 v0n = *(const u32x4*)((t < 7 ? sl + (t + 1) * TS : sx) + va0);
      const u32x4 v1n = *(const u32x4*)((t < 7 ? sl + (t + 1) * TS : sx) + va1);
      const f32x16 sn = MFMA(kf, qf, negb);
      SB();
      attn_tile(s, v0, v1, o);
      s = sn; kf = kn; v0 = v0n; v1 = v1n;
    }
  }
  TS_(2, 9);
  float l = __shfl(o[8], r);
  if (__builtin_amdgcn_ballot_w64(!(l >= 8.0e-25f && l < 3.0e38f)) != 0ull) {
    f32x16 z;
#pragma unroll
    for (int i = 0; i < 16; ++i) z[i] = 0.f;
    float m = -3.0e38f;
    for (int t = 0; t < 64; ++t) {
      const f32x16 s = MFMA(ldg16(kvh + t * 2048 + lane * 16), qf, z);
#pragma unroll
      for (int i = 0; i < 16; ++i) m = fmaxf(m, s[i]);
    }
    m = fmaxf(m, xhalf(m));
#pragma unroll
    for (int i = 0; i < 16; ++i) { negb[i] = -m; o[i] = 0.f; }
    const u32x4 ones = {0x3f803f80u, 0x3f803f80u, 0x3f803f80u, 0x3f803f80u};
    for (int t = 0; t < 64; ++t) {
      const f32x16 s = MFMA(ldg16(kvh + t * 2048 + lane * 16), qf, negb);
      u32x4 v0 = ldg16(kvh + t * 2048 + 1024 + h * 256 + (r & 15) * 16);
      u32x4 v1 = ldg16(kvh + t * 2048 + 1536 + h * 256 + (r & 15) * 16);
      if (r == 16) { v0 = ones; v1 = ones; }
      attn_tile(s, v0, v1, o);
    }
    l = __shfl(o[8], r);
  }
  *(u32x4*)(ws + O_OF + (size_t)(qt * 32 + head) * 1024 + lane * 16) = pack8(o, 0, 1.0f / l);
  TS_(2, 10);
}

constexpr int D = 6;
template <int NFT>
DI void phase1(char* sm, rsrc_t rs, unsigned w3p, unsigned w4p, u32x4 (&ring)[D][2], int w, int lane) {
  f32x16 acc[NFT][2];
#pragma unroll
  for (int t = 0; t < 2 * NFT; ++t)
#pragma unroll
    for (int i = 0; i < 16; ++i) acc[t >> 1][t & 1][i] = 0.f;
  const char* xb = sm + L_X1 + lane * 16;
  u32x4 n0 = *(const u32x4*)xb, n1 = *(const u32x4*)(xb + 32768);
#pragma unroll
  for (int k = 0; k < 32; ++k) {
    const u32x4 b0 = n0, b1 = n1;
    if (k < 31) { n0 = *(const u32x4*)(xb + (k + 1) * 1024); n1 = *(const u32x4*)(xb + 32768 + (k + 1) * 1024); }
    const u32x4 a0 = ring[k % D][0], a1 = ring[k % D][1];
    acc[0][0] = MFMA(a0, b0, acc[0][0]);
    acc[0][1] = MFMA(a0, b1, acc[0][1]);
    if (NFT == 2) { acc[NFT - 1][0] = MFMA(a1, b0, acc[NFT - 1][0]); acc[NFT - 1][1] = MFMA(a1, b1, acc[NFT - 1][1]); }
    if (k + D < 32) { ring[k % D][0] = bld(w3p + (k + D) * 1024); if (NFT == 2) ring[k % D][1] = bld(w3p + 32768 + (k + D) * 1024); }
    else { ring[k % D][0] = bld(w4p + (k % D) * 1024); ring[k % D][1] = bld(w4p + 112 * 1024 + (k % D) * 1024); }
    __builtin_amdgcn_sched_barrier(0);
  }
#pragma unroll
  for (int f = 0; f < NFT; ++f)
#pragma unroll
    for (int rt = 0; rt < 2; ++rt) {
      f32x16 o;
#pragma unroll
      for (int i = 0; i < 16; ++i) o[i] = gelu(acc[f][rt][i]);
      char* p = sm + L_H3 + (rt * 14 + (2 * w + f) * 2) * 1024 + lane * 16;
      *(u32x4*)p = pack8(o, 0, 1.0f);
      *(u32x4*)(p + 1024) = pack8(o, 8, 1.0f);
    }
}

DI void mlp_wave(char* sm, const char* x, char* ws, int rb, int j, int w, int lane) {
  const int r = lane & 31, h = lane >> 5;
  LAS unsigned* cnt = (LAS unsigned*)(sm + L_CNT);
  const rsrc_t rs = ws_rsrc(ws);
  {
    const int nt = w & 1, rt = w >> 1;
    const char* xp = x + (size_t)(rb * 64 + rt * 32 + r) * 1024 + (j * 64 + nt * 32 + 4 * h) * 2;
    u32x2 xv[4];
#pragma unroll
    for (int g = 0; g < 4; ++g) xv[g] = *(const u32x2*)(xp + g * 16);
    f32x16 o = gemm64<false>(rs, O_W2 + j * 65536, O_HS + rb * 65536, sm + L_X1, cnt + 8, w, lane);
    TS_(2, 1);
    float s1 = 0.f, s2 = 0.f;
#pragma unroll
    for (int g = 0; g < 4; ++g) {
      o[4 * g] += lo16(xv[g][0]); o[4 * g + 1] += hi16(xv[g][0]); o[4 * g + 2] += lo16(xv[g][1]); o[4 * g + 3] += hi16(xv[g][1]);
    }
    const u32x4 p0 = pack8(o, 0, 1.0f), p1 = pack8(o, 8, 1.0f);
#pragma unroll
    for (int d = 0; d < 4; ++d) {
      s1 = dot2(p0[d], 0x3f803f80u, s1); s2 = dot2(p0[d], p0[d], s2);
      s1 = dot2(p1[d], 0x3f803f80u, s1); s2 = dot2(p1[d], p1[d], s2);
    }
    s1 += xhalf(s1); s2 += xhalf(s2);
    const unsigned fo = O_X1 + ((rb * 2 + rt) * 32 + j * 4 + nt * 2) * 1024;
    __builtin_amdgcn_raw_buffer_store_b128(p0, rs, lane * 16, fo, 16);
    __builtin_amdgcn_raw_buffer_store_b128(p1, rs, lane * 16, fo + 1024, 16);
    if (h == 0) {
      const u32x2 st = {__float_as_uint(s1), __float_as_uint(s2)};
      __builtin_amdgcn_raw_buffer_store_b64(st, rs, r * 8, O_STAT + ((rb * 2 + rt) * 16 + j * 2 + nt) * 256, 16);
    }
    WAIT_VM(0);
    if (lane == 0) __hip_atomic_fetch_add((unsigned*)(ws + O_CNT + rb * 256), 1u, __ATOMIC_RELAXED, __HIP_MEMORY_SCOPE_AGENT);
  }
  TS_(2, 2);
  const unsigned w3p = O_W3 + (j * 7 + 2 * w) * 32768;
  const int f1 = w < 3 ? 32768 : 0;
  u32x4 ring[D][2];
#pragma unroll
  for (int k = 0; k < D; ++k) { ring[k][0] = bld(w3p + k * 1024); ring[k][1] = bld(w3p + f1 + k * 1024); }
  {
    unsigned* gc = (unsigned*)(ws + O_CNT + rb * 256);
    while (__builtin_amdgcn_readfirstlane(__hip_atomic_load(gc, __ATOMIC_RELAXED, __HIP_MEMORY_SCOPE_AGENT)) < 32u) __builtin_amdgcn_s_sleep(2);
    asm volatile("" ::: "memory");
    TS_(2, 3);
    const int rt = w >> 1, sb = (w & 1) * 16;
    float s1 = 0.f, s2 = 0.f;
    u32x2 st[8];
#pragma unroll
    for (int k = 0; k < 8; ++k)
      st[k] = __builtin_amdgcn_raw_buffer_load_b64(rs, h * 2048 + r * 8, O_STAT + ((rb * 2 + rt) * 16 + k) * 256, 16);
    const unsigned fo = O_X1 + ((rb * 2 + rt) * 32 + sb) * 1024;
    char* dst = sm + L_X1 + (rt * 32 + sb) * 1024 + lane * 16;
    u32x4 xf[16];
#pragma unroll
    for (int k = 0; k < 16; ++k) xf[k] = __builtin_amdgcn_raw_buffer_load_b128(rs, lane * 16, fo + k * 1024, 16);
#pragma unroll
    for (int k = 0; k < 8; ++k) { s1 += __uint_as_float(st[k][0]); s2 += __uint_as_float(st[k][1]); }
    s1 += xhalf(s1); s2 += xhalf(s2);
    const float mean = s1 * (1.0f / 512.0f);
    const float rstd = rsqrtf(fmaxf(s2 * (1.0f / 512.0f) - mean * mean, 0.f) + 1e-5f), nm = -mean * rstd;
#pragma unroll
    for (int hf = 0; hf < 2; ++hf) {
#pragma unroll
      for (int k = 0; k < 8; ++k) {
        u32x4 o;
#pragma unroll
        for (int d = 0; d < 4; ++d) o[d] = pk2(__builtin_fmaf(lo16(xf[hf * 8 + k][d]), rstd, nm), __builtin_fmaf(hi16(xf[hf * 8 + k][d]), rstd, nm));
        *(u32x4*)(dst + (hf * 8 + k) * 1024) = o;
      }
    }
    TS_(2, 4);
    lds_arrive(cnt + 9, lane);
    lds_wait(cnt + 9, 4);
    TS_(2, 5);
  }
  f32x16 acc[2][2];
  const unsigned w4p = O_W4 + (4 * w * 112 + j * 14) * 1024;
  {
    if (w < 3) phase1<2>(sm, rs, w3p, w4p, ring, w, lane); else phase1<1>(sm, rs, w3p, w4p, ring, w, lane);
    TS_(2, 6);
    lds_arrive(cnt + 10, lane);
    lds_wait(cnt + 10, 4);
    TS_(2, 7);
  }
  {
    const char* hb = sm + L_H3 + lane * 16;
    u32x4 n0 = *(const u32x4*)hb, n1 = *(const u32x4*)(hb + 14336);
#pragma unroll
    for (int ps = 0; ps < 2; ++ps) {
#pragma unroll
      for (int t = 0; t < 4; ++t)
#pragma unroll
        for (int i = 0; i < 16; ++i) acc[t >> 1][t & 1][i] = 0.f;
#pragma unroll
      for (int k = 0; k < 14; ++k) {
        const int kk = ps * 14 + k, kn = kk + D;
        const u32x4 b0 = n0, b1 = n1;
        if (kk < 27) { n0 = *(const u32x4*)(hb + ((k + 1) % 14) * 1024); n1 = *(const u32x4*)(hb + 14336 + ((k + 1) % 14) * 1024); }
        const u32x4 a0 = ring[kk % D][0], a1 = ring[kk % D][1];
        acc[0][0] = MFMA(a0, b0, acc[0][0]);
        acc[0][1] = MFMA(a0, b1, acc[0][1]);
        acc[1][0] = MFMA(a1, b0, acc[1][0]);
        acc[1][1] = MFMA(a1, b1, acc[1][1]);
        if (kn < 28) {
          const unsigned p = w4p + ((kn / 14) * 224 + kn % 14) * 1024;
          ring[kk % D][0] = bld(p); ring[kk % D][1] = bld(p + 112 * 1024);
        }
        __builtin_amdgcn_sched_barrier(0);
      }
#pragma unroll
      for (int nt = 0; nt < 2; ++nt)
#pragma unroll
        for (int rt = 0; rt < 2; ++rt) {
          const unsigned p = O_PART + j * 2 * MB + ((rb * 2 + rt) * 32 + (4 * w + 2 * ps + nt) * 2) * 1024;
          __builtin_amdgcn_raw_buffer_store_b128(pack8(acc[nt][rt], 0, 1.0f), rs, lane * 16, p, 0);
          __builtin_amdgcn_raw_buffer_store_b128(pack8(acc[nt][rt], 8, 1.0f), rs, lane * 16, p + 1024, 0);
        }
      __builtin_amdgcn_sched_barrier(0);
      TS_(2, 8 + ps);
    }
  }
}

__global__ __launch_bounds__(768, 3) void k2(const char* __restrict__ x, char* __restrict__ ws) {
  extern __shared__ __attribute__((aligned(16))) char sm[];
  const int tid = threadIdx.x, lane = tid & 63, w = __builtin_amdgcn_readfirstlane(tid >> 6);
  const int b = blockIdx.x, xcd = b & 7, i = b >> 3;
  const int head = xcd * 4 + (i >> 3), qb = i & 7;
  TS_(2, 0);
  if (w < 8) {
    const unsigned smb = (unsigned)(size_t)(LAS char*)sm;
    const char* kvg = ws + O_KV + (size_t)head * 131072 + w * 2048 + lane * 16;
    dma16(kvg, smb + w * TS);
    dma16(kvg + 1024, smb + w * TS + 1024);
    const u32x4 qf = ldg16(ws + O_QF + (size_t)(head * 64 + qb * 8 + w) * 1024 + lane * 16);
    const float km = ((const float*)(ws + O_KMAX))[head * 64 + lane];
    if (lane < 4) *(u32x4*)(sm + lane * SLOT + w * TS + 2048) = u32x4{0x3f803f80u, 0x3f803f80u, 0x3f803f80u, 0x3f803f80u};
    WAIT_LGKM();
    __builtin_amdgcn_s_barrier();
    asm volatile("" ::: "memory");
    __builtin_amdgcn_s_setprio(ATT_PRIO);
    WAIT_VM(0);
    lds_arrive((LAS unsigned*)(sm + L_CNT) + 11, lane);
    dma16(kvg + 16384, smb + SLOT + w * TS);
    dma16(kvg + 16384 + 1024, smb + SLOT + w * TS + 1024);
    attn_wave(sm, ws, head, qb, w, lane, qf, km);
  } else {
    if (w == 8 && lane < 16) *(unsigned*)(sm + L_CNT + lane * 4) = 0u;
    WAIT_LGKM();
    __builtin_amdgcn_s_barrier();
    asm volatile("" ::: "memory");
    __builtin_amdgcn_s_setprio(MLP_PRIO);
    mlp_wave(sm, x, ws, (xcd >> 1) * 8 + (i >> 2), (xcd & 1) * 4 + (i & 3), w - 8, lane);
  }
}

__global__ __launch_bounds__(256, 1) void k3(const char* __restrict__ ws, char* __restrict__ out) {
  __shared__ __attribute__((aligned(16))) char red[49152];
  const int tid = threadIdx.x, lane = tid & 63, w = __builtin_amdgcn_readfirstlane(tid >> 6);
  const int b = blockIdx.x, j = (b >> 3) & 7, rb = (b & 7) * 4 + (b >> 6);
  const int r = lane & 31, h = lane >> 5, nt = w & 1, rt = w >> 1;
  const unsigned fo = ((rb * 2 + rt) * 32 + j * 4 + nt * 2) * 1024;
  const rsrc_t rs = ws_rsrc(ws);
  u32x4 pp[9][2];
#pragma unroll
  for (int s = 0; s < 9; ++s) {
    const unsigned p = (s < 8 ? O_PART + s * 2 * MB : O_X1) + fo;
    pp[s][0] = bld(p);
    pp[s][1] = bld(p + 1024);
  }
  const float g = ((const float*)(ws + O_G))[rb * 64 + rt * 32 + r];
  const f32x16 a = gemm64<true>(rs, O_WO + j * 65536, O_OF + rb * 65536, red, nullptr, w, lane);
  f32x16 o;
#pragma unroll
  for (int i = 0; i < 16; ++i) o[i] = 0.f;
#pragma unroll
  for (int s = 0; s < 9; ++s)
#pragma unroll
    for (int f = 0; f < 2; ++f)
#pragma unroll
      for (int d = 0; d < 4; ++d) { o[8 * f + 2 * d] += lo16(pp[s][f][d]); o[8 * f + 2 * d + 1] += hi16(pp[s][f][d]); }
  char* op = out + (size_t)(rb * 64 + rt * 32 + r) * 1024 + (j * 64 + nt * 32 + 4 * h) * 2;
#pragma unroll
  for (int q = 0; q < 4; ++q) {
    float v[4];
#pragma unroll
    for (int e = 0; e < 4; ++e) v[e] = g * a[4 * q + e] + (1.0f - g) * o[4 * q + e];
    *(u32x2*)(op + q * 16) = u32x2{pk2(v[0], v[1]), pk2(v[2], v[3])};
  }
}

extern "C" void kernel_launch(void* const* d_in, const int* in_sizes, int n_in, void* d_out, int out_size, void* d_ws, size_t ws_size,
                              hipStream_t stream) {
  (void)hipFuncSetAttribute((const void*)k1, hipFuncAttributeMaxDynamicSharedMemorySize, K1_LDS);
  (void)hipFuncSetAttribute((const void*)k2, hipFuncAttributeMaxDynamicSharedMemorySize, K2_LDS);
  char* ws = (char*)d_ws;
  k1<<<256, 256, K1_LDS, stream>>>((const char*)d_in[0], (const char*)d_in[1], (const char*)d_in[4], (const char*)d_in[2], (const char*)d_in[5],
                                   (const char*)d_in[6], (const char*)d_in[7], (const char*)d_in[8], ws);
  k2<<<256, 768, K2_LDS, stream>>>((const char*)d_in[0], ws);
  k3<<<256, 256, 0, stream>>>(ws, (char*)d_out);
}
```

```cpp
#include <hip/hip_runtime.h>
#include <hip/hip_bf16.h>

typedef __attribute__((ext_vector_type(8))) __bf16 bf16x8;
typedef __attribute__((ext_vector_type(2))) __bf16 bf16x2;
typedef __attribute__((ext_vector_type(16))) float f32x16;
typedef __attribute__((ext_vector_type(4))) float f32x4;
typedef __attribute__((ext_vector_type(2))) float f32x2;
typedef __attribute__((ext_vector_type(4))) unsigned u32x4;
typedef __attribute__((ext_vector_type(2))) unsigned u32x2;
typedef __attribute__((ext_vector_type(4))) short s16x4;
#define DI __device__ __forceinline__
#define LAS __attribute__((address_space(3)))
#define MFMA(a, b, c) __builtin_amdgcn_mfma_f32_32x32x16_bf16(__builtin_bit_cast(bf16x8, a), __builtin_bit_cast(bf16x8, b), c, 0, 0, 0)

constexpr unsigned MB = 1u << 20;
constexpr unsigned O_QF = 0, O_KV = 2 * MB, O_HS = 6 * MB, O_W2 = 8 * MB, O_WO = 8 * MB + MB / 2, O_W3 = 9 * MB, O_W4 = 11 * MB,
                 O_X1 = 13 * MB, O_OF = 15 * MB, O_PART = 17 * MB, O_STAT = 33 * MB, O_KMAX = 34 * MB, O_G = 34 * MB + 16384,
                 O_CNT = 34 * MB + 32768;

DI unsigned pk2(float a, float b) { f32x2 v = {a, b}; return __builtin_bit_cast(unsigned, __builtin_convertvector(v, bf16x2)); }
DI float lo16(unsigned u) { return __uint_as_float(u << 16); }
DI float hi16(unsigned u) { return __uint_as_float(u & 0xffff0000u); }
DI float dot2(unsigned a, unsigned b, float c) { return __builtin_amdgcn_fdot2_f32_bf16(__builtin_bit_cast(bf16x2, a), __builtin_bit_cast(bf16x2, b), c, false); }
DI float xhalf(float v) { return __shfl_xor(v, 32); }
DI float gelu(float x) {
  float z = x * __builtin_fmaf(x * x, -0.10294324f, -2.3022082f);
  return x * __builtin_amdgcn_rcpf(1.0f + __builtin_amdgcn_exp2f(z));
}
DI void dma16(const void* g, unsigned lds_base) {
  unsigned sv;
  asm volatile("s_mov_b32 %0, m0\n\ts_mov_b32 m0, %2\n\ts_nop 0\n\tglobal_load_lds_dwordx4 %1, off\n\ts_mov_b32 m0, %0"
               : "=&s"(sv) : "v"(g), "s"(lds_base) : "memory");
}
#define WAIT_VM(n) asm volatile("s_waitcnt vmcnt(" #n ")" ::: "memory")
#define WAIT_LGKM() asm volatile("s_waitcnt lgkmcnt(0)" ::: "memory")
DI u32x4 pack8(const f32x16& a, int o, float m) {
  u32x4 r = {pk2(a[o] * m, a[o + 1] * m), pk2(a[o + 2] * m, a[o + 3] * m), pk2(a[o + 4] * m, a[o + 5] * m), pk2(a[o + 6] * m, a[o + 7] * m)};
  return r;
}
#define TS_(kid, k)
DI u32x4 ldg16(const char* p) { return *(const u32x4*)p; }
typedef __amdgpu_buffer_rsrc_t rsrc_t;
DI rsrc_t ws_rsrc(const char* ws) { return __builtin_amdgcn_make_buffer_rsrc((void*)ws, (short)0, (int)(35 * MB), 0x00020000); }
#define bld(soff) __builtin_amdgcn_raw_buffer_load_b128(rs, lane * 16, (unsigned)(soff), 0)

constexpr int K1_LDS = 4 * 32768 + 1024 + 1024;

template <int ROLE>
DI void k1_body(char* sm, const char* x, const char* W, int ldw, char* ws, int bm, int bn, int w, int lane, int tid, const char* ssrc, int sld, char* sd, bool side) {
  const int wn = w & 1, wm = w >> 1, r = lane & 31, h = lane >> 5;
  const unsigned smb = (unsigned)(size_t)(LAS char*)sm;
  const int xr = w * 32 + (lane >> 3);
  const char* xg = x + (size_t)(bm * 128 + xr) * 1024;
  const int wr = w * 16 + (lane >> 4);
  const char* wg = W + (size_t)wr * ldw;
  const int rot = (bm + bn) & 7;
  auto issue = [&](int c) {
    const unsigned sb = smb + (c & 3) * 32768 + w * 4096;
    const int cc = (c + rot) & 7;
#pragma unroll
    for (int p = 0; p < 4; ++p) {
      const int row = xr + 8 * p;
      dma16(xg + p * 8192 + cc * 128 + (((lane & 7) ^ ((row >> 1) & 7)) << 4), sb + p * 1024);
    }
#pragma unroll
    for (int p = 0; p < 4; ++p) {
      const int row = wr + 4 * p;
      dma16(wg + (size_t)(cc * 64 + 4 * p) * ldw + (((lane & 15) ^ (((row & 3) << 2) | ((row >> 2) & 3))) << 4), sb + 16384 + p * 1024);
    }
  };
  TS_(1, 0);
  u32x4 in[8];
  if (ROLE != 2) {
#pragma unroll
    for (int j = 0; j < 8; ++j) in[j] = ldg16(ssrc + (size_t)(8 * (j >> 2) + (j & 3)) * sld);
  }
  issue(0);
  issue(1);
  issue(2);
  issue(3);
  f32x16 acc[2][2];
#pragma unroll
  for (int a = 0; a < 2; ++a)
#pragma unroll
    for (int b = 0; b < 2; ++b)
#pragma unroll
      for (int i = 0; i < 16; ++i) acc[a][b][i] = 0.f;
  float s1[4] = {0.f, 0.f, 0.f, 0.f}, s2[4] = {0.f, 0.f, 0.f, 0.f}, cs[4] = {0.f, 0.f, 0.f, 0.f};
  const int xsw = (r >> 1) & 7;
  const int blk = (lane >> 4) & 1, q = (lane & 15) >> 2, pp = lane & 3;
  auto rd = [&](int c, int ks, u32x4 (&xf)[2], u32x4 (&wf)[2], u32x4& gwv) {
    const char* xs = sm + (c & 3) * 32768;
    const char* wsm = xs + 16384;
#pragma unroll
    for (int b = 0; b < 2; ++b) xf[b] = *(const u32x4*)(xs + (wm * 64 + (b ^ wn) * 32 + r) * 128 + (((2 * ks + h) ^ xsw) << 4));
#pragma unroll
    for (int a = 0; a < 2; ++a)
#pragma unroll
      for (int t = 0; t < 2; ++t) {
        const int row = 16 * ks + 8 * h + 4 * t + q;
        const int ch = 4 * (wn * 2 + (a ^ wm)) + 2 * blk + (pp >> 1);
        const int off = 256 * row + 16 * (ch ^ (((row & 3) << 2) | ((row >> 2) & 3))) + 8 * (pp & 1);
        u32x2 v = __builtin_bit_cast(u32x2, __builtin_amdgcn_ds_read_tr16_b64_v4i16((LAS s16x4*)(wsm + off)));
        wf[a][2 * t] = v[0];
        wf[a][2 * t + 1] = v[1];
      }
    if (ROLE == 3) gwv = *(const u32x4*)(sm + 131072 + ((c + rot) & 7) * 128 + ks * 32 + h * 16);
  };
  u32x4 xn[2], wnx[2], gn = {0u, 0u, 0u, 0u};
  WAIT_VM(24);
  __builtin_amdgcn_s_barrier();
  asm volatile("" ::: "memory");
  rd(0, 0, xn, wnx, gn);
  for (int c = 0; c < 8; ++c) {
    TS_(1, 1 + c);
#pragma unroll
    for (int ks = 0; ks < 4; ++ks) {
      const u32x4 xf[2] = {xn[0], xn[1]}, wf[2] = {wnx[0], wnx[1]}, gw = gn;
      if (ks < 3) {
        rd(c, ks + 1, xn, wnx, gn);
      } else if (c < 7) {
        WAIT_LGKM();
        if (c < 5) WAIT_VM(16); else if (c == 5) WAIT_VM(8); else WAIT_VM(0);
        __builtin_amdgcn_s_barrier();
        asm volatile("" ::: "memory");
        rd(c + 1, 0, xn, wnx, gn);
        if (c < 4) issue(c + 4);
      }
      __builtin_amdgcn_sched_barrier(0);
#pragma unroll
      for (int a = 0; a < 2; ++a)
#pragma unroll
        for (int b = 0; b < 2; ++b) acc[a][b] = ROLE == 1 ? MFMA(xf[b], wf[a], acc[a][b]) : MFMA(wf[a], xf[b], acc[a][b]);
      if (ROLE == 2) {
#pragma unroll
        for (int d = 0; d < 4; ++d) {
          s1[d] = dot2(xf[0][d], 0x3f803f80u, s1[d]);
          s2[d] = dot2(xf[0][d], xf[0][d], s2[d]);
          cs[d] = dot2(wf[0][d], 0x3f803f80u, cs[d]);
        }
#pragma unroll
        for (int m = 0; m < 4; ++m) { __builtin_amdgcn_sched_group_barrier(0x008, 1, 0); __builtin_amdgcn_sched_group_barrier(0x002, 3, 0); }
      }
      if (ROLE == 3) {
#pragma unroll
        for (int d = 0; d < 4; ++d) s1[d] = dot2(xf[0][d], gw[d], s1[d]);
#pragma unroll
        for (int m = 0; m < 4; ++m) { __builtin_amdgcn_sched_group_barrier(0x008, 1, 0); __builtin_amdgcn_sched_group_barrier(0x002, 1, 0); }
      }
      __builtin_amdgcn_sched_barrier(0);
    }
  }
  TS_(1, 9);
  if (ROLE != 2 && side) {
#pragma unroll
    for (int n = 0; n < 8; ++n) {
      u32x4 o;
#pragma unroll
      for (int dd = 0; dd < 4; ++dd) o[dd] = __builtin_amdgcn_perm(in[2 * dd + 1][n >> 1], in[2 * dd][n >> 1], (n & 1) ? 0x07060302u : 0x05040100u);
      *(u32x4*)(sd + n * 16) = o;
    }
  }
  const int tile0 = bm * 4 + wm * 2;
  if (ROLE == 0 || ROLE == 3) {
    const bool isq = bn < 4;
    const float m = isq ? 0.25f * 1.4426950409f : 1.0f;
    const int head0 = (bn & 3) * 8 + wn * 4;
    float kn[2][2][2];
#pragma unroll
    for (int nt = 0; nt < 2; ++nt)
#pragma unroll
      for (int rt = 0; rt < 2; ++rt)
#pragma unroll
        for (int s = 0; s < 2; ++s) {
          const int head = head0 + (nt ^ wm) * 2 + s;
          const int tile = tile0 + (rt ^ wn);
          const u32x4 v = pack8(acc[nt][rt], 8 * s, m);
          if (isq) {
            *(u32x4*)(ws + O_QF + (size_t)(head * 64 + tile) * 1024 + lane * 16) = v;
          } else {
            *(u32x4*)(ws + O_KV + (size_t)(head * 64 + tile) * 2048 + lane * 16) = v;
            const float n2 = dot2(v[0], v[0], dot2(v[1], v[1], 0.f)) + dot2(v[2], v[2], dot2(v[3], v[3], 0.f));
            kn[nt][rt][s] = n2 + xhalf(n2);
          }
        }
    if (!isq) {
      float km[4];
#pragma unroll
      for (int e = 0; e < 4; ++e) km[e] = fmaxf(kn[e >> 1][0][e & 1], kn[e >> 1][1][e & 1]);
#pragma unroll
      for (int o = 16; o >= 1; o >>= 1)
#pragma unroll
        for (int e = 0; e < 4; ++e) km[e] = fmaxf(km[e], __shfl_xor(km[e], o));
      if (lane < 2) {
#pragma unroll
        for (int e = 0; e < 4; ++e) ((float*)(ws + O_KMAX))[(head0 + ((e >> 1) ^ wm) * 2 + (e & 1)) * 64 + tile0 + lane] = km[e];
      }
    }
    if (ROLE == 3) {
      float z = (s1[0] + s1[1]) + (s1[2] + s1[3]);
      z += xhalf(z);
      if (h == 0) ((float*)(ws + O_G))[(tile0 + wn) * 32 + r] = 1.0f / (1.0f + __expf(-z));
    }
  } else if (ROLE == 1) {
#pragma unroll
    for (int nt = 0; nt < 2; ++nt)
#pragma unroll
      for (int rt = 0; rt < 2; ++rt)
#pragma unroll
        for (int s = 0; s < 2; ++s) {
          const int head = (bn & 3) * 8 + wn * 4 + (nt ^ wm) * 2 + (r >> 4);
          *(u32x4*)(ws + O_KV + (size_t)(head * 64 + tile0 + (rt ^ wn)) * 2048 + 1024 + s * 512 + h * 256 + (r & 15) * 16) = pack8(acc[nt][rt], 8 * s, 1.0f);
        }
  } else {
    float* cl = (float*)(sm + 131072);
    {
      float t = (cs[0] + cs[1]) + (cs[2] + cs[3]), u1 = (s1[0] + s1[1]) + (s1[2] + s1[3]), u2 = (s2[0] + s2[1]) + (s2[2] + s2[3]);
      t += xhalf(t); u1 += xhalf(u1); u2 += xhalf(u2);
      if (h == 0) { cl[wn * 64 + wm * 32 + r] = t; cl[128 + wm * 64 + wn * 32 + r] = u1; cl[256 + wm * 64 + wn * 32 + r] = u2; }
      WAIT_LGKM();
      __builtin_amdgcn_s_barrier();
      asm volatile("" ::: "memory");
    }
    const int g = bn & 3;
    const int sh = (g & 1) ? -(1 + (g >> 1)) : (1 + (g >> 1));
#pragma unroll
    for (int rt = 0; rt < 2; ++rt) {
      const float mean = cl[128 + wm * 64 + (rt ^ wn) * 32 + r] * (1.0f / 512.0f);
      const float var = fmaxf(cl[256 + wm * 64 + (rt ^ wn) * 32 + r] * (1.0f / 512.0f) - mean * mean, 0.f);
      const float rstd = rsqrtf(var + 1e-5f);
      const float nm = -mean * rstd;
      const int rd = (tile0 + (rt ^ wn)) * 32 + r + sh;
      const bool ok = rd >= 0 && rd < 2048;
      const int rw = rd & 2047;
#pragma unroll
      for (int nt = 0; nt < 2; ++nt) {
        f32x16 o;
#pragma unroll
        for (int gq = 0; gq < 4; ++gq) {
          const f32x4 c4 = *(const f32x4*)(cl + wn * 64 + (nt ^ wm) * 32 + 8 * gq + 4 * h);
#pragma unroll
          for (int e = 0; e < 4; ++e) o[4 * gq + e] = gelu(__builtin_fmaf(acc[nt][rt][4 * gq + e], rstd, nm * c4[e]));
        }
#pragma unroll
        for (int s = 0; s < 2; ++s) {
          u32x4 v = pack8(o, 8 * s, 1.0f);
          if (!ok) v = u32x4{0u, 0u, 0u, 0u};
          *(u32x4*)(ws + O_HS + (size_t)((rw >> 5) * 32 + g * 8 + wn * 4 + (nt ^ wm) * 2 + s) * 1024 + ((rw & 31) + 32 * h) * 16) = v;
        }
      }
    }
  }
}

__global__ __launch_bounds__(256, 1) void k1(const char* __restrict__ x, const char* __restrict__ wqkv, const char* __restrict__ w1,
                                             const char* __restrict__ wo, const char* __restrict__ w2, const char* __restrict__ w3,
                                             const char* __restrict__ w4, const char* __restrict__ gw, char* __restrict__ ws) {
  extern __shared__ __attribute__((aligned(16))) char sm[];
  const int tid = threadIdx.x, lane = tid & 63, w = __builtin_amdgcn_readfirstlane(tid >> 6);
  const int b = blockIdx.x, xcd = b & 7, i = b >> 3;
  const int bm = (xcd >> 1) * 4 + (i & 3), bn = (xcd & 1) * 8 + (i >> 2);
  const char* W = bn < 12 ? wqkv + bn * 256 : w1 + (bn - 12) * 256;
  const int ldw = bn < 12 ? 3072 : 1024;
  const unsigned smb = (unsigned)(size_t)(LAS char*)sm;
  const int idx = min((xcd >> 1) * 48 + (xcd & 1) * 32 + i, 191);
  const int uu = idx * 3 + max(w - 1, 0);
  const bool side = w != 0;
  const char* ssrc; char* d; int sld;
  {
    const char* src; char* dst; int ks, nb, nks;
    if (uu < 64) { src = w2; dst = ws + O_W2; sld = 1024; ks = uu >> 1; nb = uu & 1; nks = 32; }
    else if (uu < 128) { src = wo; dst = ws + O_WO; sld = 1024; ks = (uu - 64) >> 1; nb = uu & 1; nks = 32; }
    else if (uu < 352) { src = w3; dst = ws + O_W3; sld = 3584; ks = (uu - 128) / 7; nb = (uu - 128) % 7; nks = 32; }
    else { src = w4; dst = ws + O_W4; sld = 1024; ks = (uu - 352) >> 1; nb = uu & 1; nks = 112; }
    const int r = lane & 31, h = lane >> 5;
    ssrc = src + (size_t)(ks * 16 + 4 * h) * sld + nb * 512 + r * 16;
    d = dst + (size_t)((nb * 8 + (r >> 2)) * nks + ks) * 1024 + ((r & 3) * 8 + 32 * h) * 16;
  }
  if (b == 0 && tid < 32) __hip_atomic_store((unsigned*)(ws + O_CNT + tid * 256), 0u, __ATOMIC_RELAXED, __HIP_MEMORY_SCOPE_AGENT);
  if (w == 0 && bn == 0) dma16(gw + lane * 16, smb + 131072);
  if (bn == 0) k1_body<3>(sm, x, W, ldw, ws, bm, bn, w, lane, tid, ssrc, sld, d, side);
  else if (bn < 8) k1_body<0>(sm, x, W, ldw, ws, bm, bn, w, lane, tid, ssrc, sld, d, side);
  else if (bn < 12) k1_body<1>(sm, x, W, ldw, ws, bm, bn, w, lane, tid, ssrc, sld, d, side);
  else k1_body<2>(sm, x, W, ldw, ws, bm, bn, w, lane, tid, ssrc, sld, d, side);
  TS_(1, 10);
  TS_(1, 11);
}

DI void lds_add(LAS unsigned* p) { __hip_atomic_fetch_add(p, 1u, __ATOMIC_RELAXED, __HIP_MEMORY_SCOPE_WORKGROUP); }
DI void lds_wait(LAS unsigned* p, unsigned want) {
  while (__builtin_amdgcn_readfirstlane(__hip_atomic_load(p, __ATOMIC_RELAXED, __HIP_MEMORY_SCOPE_WORKGROUP)) < want) __builtin_amdgcn_s_sleep(1);
  asm volatile("" ::: "memory");
}
DI void lds_arrive(LAS unsigned* p, int lane) {
  WAIT_LGKM();
  if (lane == 0) lds_add(p);
  asm volatile("" ::: "memory");
}

template <bool BAR>
DI f32x16 gemm64(rsrc_t rs, unsigned A, unsigned B, char* red, LAS unsigned* cnt, int w, int lane) {
  const unsigned a0 = A + w * 8192 + (w & 1) * 32768, a1 = A + w * 8192 + ((w & 1) ^ 1) * 32768;
  const unsigned b0 = B + w * 8192 + (w >> 1) * 32768, b1 = B + w * 8192 + ((w >> 1) ^ 1) * 32768;
  f32x16 acc[4];
#pragma unroll
  for (int t = 0; t < 4; ++t)
#pragma unroll
    for (int i = 0; i < 16; ++i) acc[t][i] = 0.f;
  u32x4 af[8][2], bf[8][2];
#pragma unroll
  for (int k = 0; k < 8; ++k) {
    if (!BAR && k == 5) __builtin_amdgcn_sched_barrier(0);
    if (!BAR && k >= 5) {
#pragma unroll
      for (int t = 0; t < 4; ++t) acc[t] = MFMA(af[k - 5][t & 1], bf[k - 5][t >> 1], acc[t]);
      __builtin_amdgcn_sched_barrier(0);
    }
    af[k][0] = bld(a0 + k * 1024); af[k][1] = bld(a1 + k * 1024);
    bf[k][0] = bld(b0 + k * 1024); bf[k][1] = bld(b1 + k * 1024);
  }
#pragma unroll
  for (int k = BAR ? 0 : 3; k < 8; ++k)
#pragma unroll
    for (int t = 0; t < 4; ++t) acc[t] = MFMA(af[k][t & 1], bf[k][t >> 1], acc[t]);
#pragma unroll
  for (int t = 1; t < 4; ++t) {
    char* p = red + ((w ^ t) * 3 + t - 1) * 4096 + lane * 16;
#pragma unroll
    for (int g = 0; g < 4; ++g) *(f32x4*)(p + g * 1024) = f32x4{acc[t][4 * g], acc[t][4 * g + 1], acc[t][4 * g + 2], acc[t][4 * g + 3]};
  }
  f32x16 o = acc[0];
  if (BAR) { __syncthreads(); } else { lds_arrive(cnt, lane); lds_wait(cnt, 4); }
#pragma unroll
  for (int dlt = 0; dlt < 3; ++dlt)
#pragma unroll
    for (int g = 0; g < 4; ++g) {
      const f32x4 v = *(const f32x4*)(red + (w * 3 + dlt) * 4096 + lane * 16 + g * 1024);
#pragma unroll
      for (int e = 0; e < 4; ++e) o[4 * g + e] += v[e];
    }
  return o;
}

#define ROT 0
#define ATT_PRIO 0
#define MLP_PRIO 1
constexpr int TS = 2064, SLOT = 8 * TS;
constexpr int L_X1 = 4 * SLOT, L_H3 = L_X1 + 65536, L_CNT = L_H3 + 28672, K2_LDS = L_CNT + 64;

#define SB() __builtin_amdgcn_sched_barrier(0)
#define GAP 7
#define GP() do { SB(); asm volatile("s_nop %0" ::"n"(GAP)); SB(); } while (0)
DI void attn_tile(const f32x16& s, const u32x4& v0, const u32x4& v1, f32x16& o) {
  f32x16 p;
#pragma unroll
  for (int i = 0; i < 8; ++i) p[i] = __builtin_amdgcn_exp2f(s[i]);
  const u32x4 p0 = pack8(p, 0, 1.0f);
  SB();
  o = MFMA(v0, p0, o);
  SB();
#pragma unroll
  for (int i = 8; i < 16; ++i) p[i] = __builtin_amdgcn_exp2f(s[i]);
  const u32x4 p1 = pack8(p, 8, 1.0f);
  SB();
  o = MFMA(v1, p1, o);
  SB();
}

DI void attn_wave(char* sm, char* ws, int head, int qb, int w, int lane, const u32x4 qf, float km) {
  const int r = lane & 31, h = lane >> 5;
  const unsigned smb = (unsigned)(size_t)(LAS char*)sm;
  LAS unsigned* cnt = (LAS unsigned*)(sm + L_CNT);
  const char* kvh = ws + O_KV + (size_t)head * 131072;
  const char* kvg = kvh + w * 2048 + lane * 16;
  const int qt = qb * 8 + w;
#pragma unroll
  for (int o = 32; o >= 1; o >>= 1) km = fmaxf(km, __shfl_xor(km, o));
  float q2 = dot2(qf[0], qf[0], dot2(qf[1], qf[1], dot2(qf[2], qf[2], dot2(qf[3], qf[3], 0.f))));
  q2 += xhalf(q2);
  const float nb = -sqrtf(q2 * km) * 1.01f;
  f32x16 negb, o;
#pragma unroll
  for (int i = 0; i < 16; ++i) { negb[i] = nb; o[i] = 0.f; }
  const int va0 = r == 16 ? 2048 : 1024 + h * 256 + (r & 15) * 16;
  const int va1 = r == 16 ? 2048 : 1536 + h * 256 + (r & 15) * 16;
  lds_wait(cnt + 11, 8);
  u32x4 kf = *(const u32x4*)(sm + TS + lane * 16), v0 = *(const u32x4*)(sm + va0), v1 = *(const u32x4*)(sm + va1);
  f32x16 s = MFMA(*(const u32x4*)(sm + lane * 16), qf, negb);
  for (int c = 0; c < 8; ++c) {
    WAIT_VM(0);
    lds_arrive(cnt + c, lane);
    if (c + 2 < 8) {
      const unsigned sb = smb + ((c + 2) & 3) * SLOT + w * TS;
      dma16(kvg + (c + 2) * 16384, sb);
      dma16(kvg + (c + 2) * 16384 + 1024, sb + 1024);
    }
    TS_(2, 1 + c);
    const char* sl = sm + (c & 3) * SLOT;
    const char* sx = sm + ((c + 1) & 3) * SLOT;
    unsigned fl = 0;
#pragma unroll
    for (int t = 0; t < 8; ++t) {
      if (t == 4) fl = __hip_atomic_load(cnt + c, __ATOMIC_RELAXED, __HIP_MEMORY_SCOPE_WORKGROUP);
      if (t == 6 && __builtin_amdgcn_readfirstlane(fl) < 8u) lds_wait(cnt + c, 8);
      const u32x4 kn = *(const u32x4*)((t < 6 ? sl + (t + 2) * TS : sx + (t - 6) * TS) + lane * 16);
      const u32x4 v0n = *(const u32x4*)((t < 7 ? sl + (t + 1) * TS : sx) + va0);
      const u32x4 v1n = *(const u32x4*)((t < 7 ? sl + (t + 1) * TS : sx) + va1);
      const f32x16 sn = MFMA(kf, qf, negb);
      SB();
      attn_tile(s, v0, v1, o);
      s = sn; kf = kn; v0 = v0n; v1 = v1n;
    }
  }
  TS_(2, 9);
  float l = __shfl(o[8], r);
  if (__builtin_amdgcn_ballot_w64(!(l >= 8.0e-25f && l < 3.0e38f)) != 0ull) {
    f32x16 z;
#pragma unroll
    for (int i = 0; i < 16; ++i) z[i] = 0.f;
    float m = -3.0e38f;
    for (int t = 0; t < 64; ++t) {
      const f32x16 s = MFMA(ldg16(kvh + t * 2048 + lane * 16), qf, z);
#pragma unroll
      for (int i = 0; i < 16; ++i) m = fmaxf(m, s[i]);
    }
    m = fmaxf(m, xhalf(m));
#pragma unroll
    for (int i = 0; i < 16; ++i) { negb[i] = -m; o[i] = 0.f; }
    const u32x4 ones = {0x3f803f80u, 0x3f803f80u, 0x3f803f80u, 0x3f803f80u};
    for (int t = 0; t < 64; ++t) {
      const f32x16 s = MFMA(ldg16(kvh + t * 2048 + lane * 16), qf, negb);
      u32x4 v0 = ldg16(kvh + t * 2048 + 1024 + h * 256 + (r & 15) * 16);
      u32x4 v1 = ldg16(kvh + t * 2048 + 1536 + h * 256 + (r & 15) * 16);
      if (r == 16) { v0 = ones; v1 = ones; }
      attn_tile(s, v0, v1, o);
    }
    l = __shfl(o[8], r);
  }
  *(u32x4*)(ws + O_OF + (size_t)(qt * 32 + head) * 1024 + lane * 16) = pack8(o, 0, 1.0f / l);
  TS_(2, 10);
}

constexpr int D = 6;
template <int NFT>
DI void phase1(char* sm, rsrc_t rs, unsigned w3p, unsigned w4p, u32x4 (&ring)[D][2], int w, int lane) {
  f32x16 acc[NFT][2];
#pragma unroll
  for (int t = 0; t < 2 * NFT; ++t)
#pragma unroll
    for (int i = 0; i < 16; ++i) acc[t >> 1][t & 1][i] = 0.f;
  const char* xb = sm + L_X1 + lane * 16;
  u32x4 n0 = *(const u32x4*)xb, n1 = *(const u32x4*)(xb + 32768);
#pragma unroll
  for (int k = 0; k < 32; ++k) {
    const u32x4 b0 = n0, b1 = n1;
    if (k < 31) { n0 = *(const u32x4*)(xb + (k + 1) * 1024); n1 = *(const u32x4*)(xb + 32768 + (k + 1) * 1024); }
    const u32x4 a0 = ring[k % D][0], a1 = ring[k % D][1];
    acc[0][0] = MFMA(a0, b0, acc[0][0]); GP();
    acc[0][1] = MFMA(a0, b1, acc[0][1]); GP();
    if (NFT == 2) { acc[NFT - 1][0] = MFMA(a1, b0, acc[NFT - 1][0]); GP(); acc[NFT - 1][1] = MFMA(a1, b1, acc[NFT - 1][1]); GP(); }
    if (k + D < 32) { ring[k % D][0] = bld(w3p + (k + D) * 1024); if (NFT == 2) ring[k % D][1] = bld(w3p + 32768 + (k + D) * 1024); }
    else { ring[k % D][0] = bld(w4p + (k % D) * 1024); ring[k % D][1] = bld(w4p + 112 * 1024 + (k % D) * 1024); }
    __builtin_amdgcn_sched_barrier(0);
  }
#pragma unroll
  for (int f = 0; f < NFT; ++f)
#pragma unroll
    for (int rt = 0; rt < 2; ++rt) {
      f32x16 o;
#pragma unroll
      for (int i = 0; i < 16; ++i) o[i] = gelu(acc[f][rt][i]);
      char* p = sm + L_H3 + (rt * 14 + (2 * w + f) * 2) * 1024 + lane * 16;
      *(u32x4*)p = pack8(o, 0, 1.0f);
      *(u32x4*)(p + 1024) = pack8(o, 8, 1.0f);
    }
}

DI void mlp_wave(char* sm, const char* x, char* ws, int rb, int j, int w, int lane) {
  const int r = lane & 31, h = lane >> 5;
  LAS unsigned* cnt = (LAS unsigned*)(sm + L_CNT);
  const rsrc_t rs = ws_rsrc(ws);
  {
    const int nt = w & 1, rt = w >> 1;
    const char* xp = x + (size_t)(rb * 64 + rt * 32 + r) * 1024 + (j * 64 + nt * 32 + 4 * h) * 2;
    u32x2 xv[4];
#pragma unroll
    for (int g = 0; g < 4; ++g) xv[g] = *(const u32x2*)(xp + g * 16);
    lds_wait(cnt + 11, 8);
    f32x16 o = gemm64<false>(rs, O_W2 + j * 65536, O_HS + rb * 65536, sm + L_X1, cnt + 8, w, lane);
    __builtin_amdgcn_s_setprio(MLP_PRIO);
    TS_(2, 1);
    float s1 = 0.f, s2 = 0.f;
#pragma unroll
    for (int g = 0; g < 4; ++g) {
      o[4 * g] += lo16(xv[g][0]); o[4 * g + 1] += hi16(xv[g][0]); o[4 * g + 2] += lo16(xv[g][1]); o[4 * g + 3] += hi16(xv[g][1]);
    }
    const u32x4 p0 = pack8(o, 0, 1.0f), p1 = pack8(o, 8, 1.0f);
#pragma unroll
    for (int d = 0; d < 4; ++d) {
      s1 = dot2(p0[d], 0x3f803f80u, s1); s2 = dot2(p0[d], p0[d], s2);
      s1 = dot2(p1[d], 0x3f803f80u, s1); s2 = dot2(p1[d], p1[d], s2);
    }
    s1 += xhalf(s1); s2 += xhalf(s2);
    const unsigned fo = O_X1 + ((rb * 2 + rt) * 32 + j * 4 + nt * 2) * 1024;
    __builtin_amdgcn_raw_buffer_store_b128(p0, rs, lane * 16, fo, 16);
    __builtin_amdgcn_raw_buffer_store_b128(p1, rs, lane * 16, fo + 1024, 16);
    if (h == 0) {
      const u32x2 st = {__float_as_uint(s1), __float_as_uint(s2)};
      __builtin_amdgcn_raw_buffer_store_b64(st, rs, r * 8, O_STAT + ((rb * 2 + rt) * 16 + j * 2 + nt) * 256, 16);
    }
    WAIT_VM(0);
    if (lane == 0) __hip_atomic_fetch_add((unsigned*)(ws + O_CNT + rb * 256), 1u, __ATOMIC_RELAXED, __HIP_MEMORY_SCOPE_AGENT);
  }
  TS_(2, 2);
  const unsigned w3p = O_W3 + (j * 7 + 2 * w) * 32768;
  const int f1 = w < 3 ? 32768 : 0;
  u32x4 ring[D][2];
#pragma unroll
  for (int k = 0; k < D; ++k) { ring[k][0] = bld(w3p + k * 1024); ring[k][1] = bld(w3p + f1 + k * 1024); }
  {
    unsigned* gc = (unsigned*)(ws + O_CNT + rb * 256);
    while (__builtin_amdgcn_readfirstlane(__hip_atomic_load(gc, __ATOMIC_RELAXED, __HIP_MEMORY_SCOPE_AGENT)) < 32u) __builtin_amdgcn_s_sleep(2);
    asm volatile("" ::: "memory");
    TS_(2, 3);
    const int rt = w >> 1, sb = (w & 1) * 16;
    float s1 = 0.f, s2 = 0.f;
    u32x2 st[8];
#pragma unroll
    for (int k = 0; k < 8; ++k)
      st[k] = __builtin_amdgcn_raw_buffer_load_b64(rs, h * 2048 + r * 8, O_STAT + ((rb * 2 + rt) * 16 + k) * 256, 16);
    const unsigned fo = O_X1 + ((rb * 2 + rt) * 32 + sb) * 1024;
    char* dst = sm + L_X1 + (rt * 32 + sb) * 1024 + lane * 16;
    u32x4 xf[16];
#pragma unroll
    for (int k = 0; k < 16; ++k) xf[k] = __builtin_amdgcn_raw_buffer_load_b128(rs, lane * 16, fo + (k ^ ROT) * 1024, 16);
#pragma unroll
    for (int k = 0; k < 8; ++k) { s1 += __uint_as_float(st[k][0]); s2 += __uint_as_float(st[k][1]); }
    s1 += xhalf(s1); s2 += xhalf(s2);
    const float mean = s1 * (1.0f / 512.0f);
    const float rstd = rsqrtf(fmaxf(s2 * (1.0f / 512.0f) - mean * mean, 0.f) + 1e-5f), nm = -mean * rstd;
#pragma unroll
    for (int hf = 0; hf < 2; ++hf) {
#pragma unroll
      for (int k = 0; k < 8; ++k) {
        u32x4 o;
#pragma unroll
        for (int d = 0; d < 4; ++d) o[d] = pk2(__builtin_fmaf(lo16(xf[hf * 8 + k][d]), rstd, nm), __builtin_fmaf(hi16(xf[hf * 8 + k][d]), rstd, nm));
        *(u32x4*)(dst + ((hf * 8 + k) ^ ROT) * 1024) = o;
      }
    }
    TS_(2, 4);
    lds_arrive(cnt + 9, lane);
    lds_wait(cnt + 9, 4);
    TS_(2, 5);
  }
  f32x16 acc[2][2];
  const unsigned w4p = O_W4 + (4 * w * 112 + j * 14) * 1024;
  {
    if (w < 3) phase1<2>(sm, rs, w3p, w4p, ring, w, lane); else phase1<1>(sm, rs, w3p, w4p, ring, w, lane);
    TS_(2, 6);
    lds_arrive(cnt + 10, lane);
    lds_wait(cnt + 10, 4);
    TS_(2, 7);
  }
  {
    const char* hb = sm + L_H3 + lane * 16;
    u32x4 n0 = *(const u32x4*)hb, n1 = *(const u32x4*)(hb + 14336);
#pragma unroll
    for (int ps = 0; ps < 2; ++ps) {
#pragma unroll
      for (int t = 0; t < 4; ++t)
#pragma unroll
        for (int i = 0; i < 16; ++i) acc[t >> 1][t & 1][i] = 0.f;
#pragma unroll
      for (int k = 0; k < 14; ++k) {
        const int kk = ps * 14 + k, kn = kk + D;
        const u32x4 b0 = n0, b1 = n1;
        if (kk < 27) { n0 = *(const u32x4*)(hb + ((k + 1) % 14) * 1024); n1 = *(const u32x4*)(hb + 14336 + ((k + 1) % 14) * 1024); }
        const u32x4 a0 = ring[kk % D][0], a1 = ring[kk % D][1];
        acc[0][0] = MFMA(a0, b0, acc[0][0]); GP();
        acc[0][1] = MFMA(a0, b1, acc[0][1]); GP();
        acc[1][0] = MFMA(a1, b0, acc[1][0]); GP();
        acc[1][1] = MFMA(a1, b1, acc[1][1]); GP();
        if (kn < 28) {
          const unsigned p = w4p + ((kn / 14) * 224 + kn % 14) * 1024;
          ring[kk % D][0] = bld(p); ring[kk % D][1] = bld(p + 112 * 1024);
        }
        __builtin_amdgcn_sched_barrier(0);
      }
#pragma unroll
      for (int nt = 0; nt < 2; ++nt)
#pragma unroll
        for (int rt = 0; rt < 2; ++rt) {
          const unsigned p = O_PART + j * 2 * MB + ((rb * 2 + rt) * 32 + (4 * w + 2 * ps + nt) * 2) * 1024;
          __builtin_amdgcn_raw_buffer_store_b128(pack8(acc[nt][rt], 0, 1.0f), rs, lane * 16, p, 0);
          __builtin_amdgcn_raw_buffer_store_b128(pack8(acc[nt][rt], 8, 1.0f), rs, lane * 16, p + 1024, 0);
        }
      __builtin_amdgcn_sched_barrier(0);
      TS_(2, 8 + ps);
    }
  }
}

__global__ __launch_bounds__(768, 3) void k2(const char* __restrict__ x, char* __restrict__ ws) {
  extern __shared__ __attribute__((aligned(16))) char sm[];
  const int tid = threadIdx.x, lane = tid & 63, w = __builtin_amdgcn_readfirstlane(tid >> 6);
  const int b = blockIdx.x, xcd = b & 7, i = b >> 3;
  const int head = xcd * 4 + (i >> 3), qb = i & 7;
  TS_(2, 0);
  if (w < 8) {
    const unsigned smb = (unsigned)(size_t)(LAS char*)sm;
    const char* kvg = ws + O_KV + (size_t)head * 131072 + w * 2048 + lane * 16;
    dma16(kvg, smb + w * TS);
    dma16(kvg + 1024, smb + w * TS + 1024);
    const u32x4 qf = ldg16(ws + O_QF + (size_t)(head * 64 + qb * 8 + w) * 1024 + lane * 16);
    const float km = ((const float*)(ws + O_KMAX))[head * 64 + lane];
    dma16(kvg + 16384, smb + SLOT + w * TS);
    dma16(kvg + 16384 + 1024, smb + SLOT + w * TS + 1024);
    if (lane < 4) *(u32x4*)(sm + lane * SLOT + w * TS + 2048) = u32x4{0x3f803f80u, 0x3f803f80u, 0x3f803f80u, 0x3f803f80u};
    WAIT_LGKM();
    __builtin_amdgcn_s_barrier();
    asm volatile("" ::: "memory");
    __builtin_amdgcn_s_setprio(ATT_PRIO);
    WAIT_VM(2);
    lds_arrive((LAS unsigned*)(sm + L_CNT) + 11, lane);
    attn_wave(sm, ws, head, qb, w, lane, qf, km);
  } else {
    if (w == 8 && lane < 16) *(unsigned*)(sm + L_CNT + lane * 4) = 0u;
    WAIT_LGKM();
    __builtin_amdgcn_s_barrier();
    asm volatile("" ::: "memory");
    mlp_wave(sm, x, ws, (xcd >> 1) * 8 + (i >> 2), (xcd & 1) * 4 + (i & 3), w - 8, lane);
  }
}

__global__ __launch_bounds__(256, 1) void k3(const char* __restrict__ ws, char* __restrict__ out) {
  __shared__ __attribute__((aligned(16))) char red[49152];
  const int tid = threadIdx.x, lane = tid & 63, w = __builtin_amdgcn_readfirstlane(tid >> 6);
  const int b = blockIdx.x, j = (b >> 3) & 7, rb = (b & 7) * 4 + (b >> 6);
  const int r = lane & 31, h = lane >> 5, nt = w & 1, rt = w >> 1;
  const unsigned fo = ((rb * 2 + rt) * 32 + j * 4 + nt * 2) * 1024;
  const rsrc_t rs = ws_rsrc(ws);
  u32x4 pp[9][2];
#pragma unroll
  for (int s = 0; s < 9; ++s) {
    const unsigned p = (s < 8 ? O_PART + s * 2 * MB : O_X1) + fo;
    pp[s][0] = bld(p);
    pp[s][1] = bld(p + 1024);
  }
  const float g = ((const float*)(ws + O_G))[rb * 64 + rt * 32 + r];
  const f32x16 a = gemm64<true>(rs, O_WO + j * 65536, O_OF + rb * 65536, red, nullptr, w, lane);
  f32x16 o;
#pragma unroll
  for (int i = 0; i < 16; ++i) o[i] = 0.f;
#pragma unroll
  for (int s = 0; s < 9; ++s)
#pragma unroll
    for (int f = 0; f < 2; ++f)
#pragma unroll
      for (int d = 0; d < 4; ++d) { o[8 * f + 2 * d] += lo16(pp[s][f][d]); o[8 * f + 2 * d + 1] += hi16(pp[s][f][d]); }
  char* op = out + (size_t)(rb * 64 + rt * 32 + r) * 1024 + (j * 64 + nt * 32 + 4 * h) * 2;
#pragma unroll
  for (int q = 0; q < 4; ++q) {
    float v[4];
#pragma unroll
    for (int e = 0; e < 4; ++e) v[e] = g * a[4 * q + e] + (1.0f - g) * o[4 * q + e];
    *(u32x2*)(op + q * 16) = u32x2{pk2(v[0], v[1]), pk2(v[2], v[3])};
  }
}

extern "C" void kernel_launch(void* const* d_in, const int* in_sizes, int n_in, void* d_out, int out_size, void* d_ws, size_t ws_size,
                              hipStream_t stream) {
  (void)hipFuncSetAttribute((const void*)k1, hipFuncAttributeMaxDynamicSharedMemorySize, K1_LDS);
  (void)hipFuncSetAttribute((const void*)k2, hipFuncAttributeMaxDynamicSharedMemorySize, K2_LDS);
  char* ws = (char*)d_ws;
  k1<<<256, 256, K1_LDS, stream>>>((const char*)d_in[0], (const char*)d_in[1], (const char*)d_in[4], (const char*)d_in[2], (const char*)d_in[5],
                                   (const char*)d_in[6], (const char*)d_in[7], (const char*)d_in[8], ws);
  k2<<<256, 768, K2_LDS, stream>>>((const char*)d_in[0], ws);
  k3<<<256, 256, 0, stream>>>(ws, (char*)d_out);
}
```
